```python
import torch

def _softmax(x, axis=-1):
    return torch.softmax(x, dim=axis)
def _gelu(x):
    return torch.nn.functional.gelu(x, approximate="tanh")
def _silu(x):
    return torch.nn.functional.silu(x)
def _sigmoid(x):
    return torch.sigmoid(x)
def _layernorm(x, axis=-1, eps=1e-5):
    m = x.mean(dim=axis, keepdim=True)
    v = x.var(dim=axis, keepdim=True, unbiased=False)
    return (x - m) / torch.sqrt(v + eps)
def _scatter_add(tgt, idx, src, axis=0):
    return tgt.clone().index_add_(axis, idx, src)
def _segment_sum(src, seg, n):
    out = torch.zeros((n,) + src.shape[1:], dtype=src.dtype)
    return out.index_add_(0, seg, src)
def _pad(x, pw):
    flat = tuple(v for pair in reversed(pw) for v in pair)
    return torch.nn.functional.pad(x, flat)

kernel_name = 'concat_heads__ssm_conv_s4__mha_block__prompt'

def setup_inputs():

    _w = (lambda a: (lambda t: t.to(torch.bfloat16) if t.is_floating_point() else t)(torch.as_tensor(a)))
    def _A_setup():
        B, L, D, N = 1, 2048, 512, 128
        A_re = -(lambda _x: torch.exp(torch.as_tensor(_x)))((lambda *s: torch.randn(*s, dtype=torch.bfloat16) * 6.4)(D, N))
        A_im = torch.pi * (lambda *s: torch.randn(*s, dtype=torch.bfloat16) * 0.1)(D, N)
        B_re = (lambda *s: torch.randn(*s, dtype=torch.bfloat16) * 0.2)(D, N)
        B_im = (lambda *s: torch.randn(*s, dtype=torch.bfloat16) * 0.2)(D, N)
        C_re = (lambda *s: torch.randn(*s, dtype=torch.bfloat16) * 0.2)(D, N)
        C_im = (lambda *s: torch.randn(*s, dtype=torch.bfloat16) * 0.2)(D, N)
        log_dt = (lambda *s: torch.randn(*s, dtype=torch.bfloat16) * 0.1)(D) * 0.5 - 4.0
        Dskip = (lambda *s: torch.randn(*s, dtype=torch.bfloat16) * 0.1)(D)
        return {
            "u": _w((lambda *s: torch.randn(*s, dtype=torch.bfloat16) * 0.05)(B, L, D)),
            "A_re": _w(A_re),
            "A_im": _w(A_im),
            "B_re": _w(B_re),
            "B_im": _w(B_im),
            "C_re": _w(C_re),
            "C_im": _w(C_im),
            "log_dt": _w(log_dt),
            "Dskip": _w(Dskip),
        }
    _A_in = _A_setup()

    def _B_setup():
        B, S, D, H = 1, 2048, 512, 32
        return {
            "x": _w((lambda *s: torch.randn(*s, dtype=torch.bfloat16) * 0.2)(B, S, D)),
            "wqkv": _w((lambda *s: torch.randn(*s, dtype=torch.bfloat16) * 0.2)(D, 3 * D)),
            "wo": _w((lambda *s: torch.randn(*s, dtype=torch.bfloat16) * 0.05)(D, D)),
        }
    _B_in = _B_setup()

    return {
        "A_u": _A_in["u"],
        "A_A_re": _A_in["A_re"],
        "A_A_im": _A_in["A_im"],
        "A_B_re": _A_in["B_re"],
        "A_B_im": _A_in["B_im"],
        "A_C_re": _A_in["C_re"],
        "A_C_im": _A_in["C_im"],
        "A_log_dt": _A_in["log_dt"],
        "A_Dskip": _A_in["Dskip"],
        "B_x": _B_in["x"],
        "B_wqkv": _B_in["wqkv"],
        "B_wo": _B_in["wo"],
    }

def reference(A_u, A_A_re, A_A_im, A_B_re, A_B_im, A_C_re, A_C_im, A_log_dt, A_Dskip, B_x, B_wqkv, B_wo):
    def _A_ref(u, A_re, A_im, B_re, B_im, C_re, C_im, log_dt, Dskip):
        Bsz, L, D = u.shape
        dt = torch.exp(log_dt)[:, None]
        A = A_re + 1j * A_im
        Bc = B_re + 1j * B_im
        Cc = C_re + 1j * C_im
        logdA = dt * A
        dB = dt * Bc
        l = (lambda x, dt: x.to(dt))(torch.arange(L), A_re.dtype)
        P = torch.exp(logdA[:, :, None] * l[None, None, :])
        K = torch.einsum("dn,dnl,dn->dl", Cc, P, dB).real
        L2 = 2 * L
        Kp = _pad(K, ((0, 0), (0, L)))
        up = (lambda x, *d: x.permute(*d))(u, 0, 2, 1)
        up = _pad(up, ((0, 0), (0, 0), (0, L)))
        Kf = (lambda x, axes=(-2,-1): torch.fft.rfft2(x.float(), dim=axes))(Kp[:, None, :], axes=(-2, -1))
        Uf = (lambda x, axes=(-2,-1): torch.fft.rfft2(x.float(), dim=axes))(up[:, :, None, :], axes=(-2, -1))
        Yf = Uf * Kf[None, :, :, :]
        y = (lambda x, s, axes=(-2,-1): torch.fft.irfft2(x, s=s, dim=axes).to(torch.bfloat16))(Yf, (1, L2), axes=(-2, -1))[:, :, 0, :L]
        y = (lambda x, *d: x.permute(*d))(y, 0, 2, 1) + u * Dskip[None, None, :]
        return y

    def _B_ref(x, wqkv, wo):
        B, S, D = x.shape
        H = 32
        Dh = D // H
        qkv = (lambda x, *d: x.permute(*d))((x @ wqkv).reshape(B, S, 3, H, Dh), 2, 0, 3, 1, 4)
        q, k, v = qkv[0], qkv[1], qkv[2]
        scores = torch.einsum("bhqd,bhkd->bhqk", q, k) * (Dh ** -0.5)
        p = _softmax(scores, axis=-1)
        o = torch.einsum("bhqk,bhkd->bhqd", p, v)
        return (lambda x, *d: x.permute(*d))(o, 0, 2, 1, 3).reshape(B, S, D) @ wo

    a = _A_ref(A_u, A_A_re, A_A_im, A_B_re, A_B_im, A_C_re, A_C_im, A_log_dt, A_Dskip)
    b = _B_ref(B_x, B_wqkv, B_wo)
    return torch.concatenate([a, b], axis=-1)
```

```cpp
#include <hip/hip_runtime.h>
#include <hip/hip_bf16.h>
typedef unsigned short u16;
__device__ __forceinline__ float b2f(u16 x){ return __uint_as_float(((unsigned)x)<<16); }
__device__ __forceinline__ u16 f2b(float f){ unsigned u=__float_as_uint(f); u += 0x7FFF + ((u>>16)&1); return (u16)(u>>16); }
#define ROUNDL 1
__global__ void kgen(const u16* Are,const u16* Aim,const u16* Bre,const u16* Bim,const u16* Cre,const u16* Cim,const u16* ldt,float* K){
  int d=blockIdx.y; int l=blockIdx.x*256+threadIdx.x;
  float dt=expf(b2f(ldt[d]));
  float lf=(float)l;
  if (ROUNDL){ dt=b2f(f2b(dt)); lf=b2f(f2b(lf)); }
  float acc=0;
  for(int n=0;n<128;n++){
    int i=d*128+n;
    float ar=dt*b2f(Are[i]), ai=dt*b2f(Aim[i]);
    float br=dt*b2f(Bre[i]), bi=dt*b2f(Bim[i]);
    float cr=b2f(Cre[i]), ci=b2f(Cim[i]);
    float wr=cr*br-ci*bi, wi=cr*bi+ci*br;
    float m=expf(ar*lf); float s,c; sincosf(ai*lf,&s,&c);
    acc += m*(wr*c-wi*s);
  }
  K[d*2048+l]=acc;
}
__global__ void conv(const u16* u,const float* K,const u16* Dsk,u16* out){
  int d=blockIdx.x*64+(threadIdx.x&63); int t=blockIdx.y*4+(threadIdx.x>>6);
  float acc=0;
  for(int s=0;s<=t;s++) acc+=K[d*2048+(t-s)]*b2f(u[s*512+d]);
  float y=b2f(f2b(acc));
  float sk=b2f(f2b(b2f(u[t*512+d])*b2f(Dsk[d])));
  out[t*1024+d]=f2b(y+sk);
}
__global__ void gemm(const u16* A,const u16* B,u16* C,int N,int ldc){
  int n=blockIdx.x*64+(threadIdx.x&63); int m=blockIdx.y*4+(threadIdx.x>>6);
  float acc=0;
  for(int k=0;k<512;k++) acc+=b2f(A[m*512+k])*b2f(B[k*N+n]);
  C[m*ldc+n]=f2b(acc);
}
__global__ void attn(const u16* qkv,u16* O){
  int h=blockIdx.y; int q=blockIdx.x*64+threadIdx.x;
  float qv[16], o[16];
  for(int d=0;d<16;d++){ qv[d]=b2f(qkv[q*1536+h*16+d])*0.25f; o[d]=0; }
  float m=-1e30f,l=0;
  for(int k=0;k<2048;k++){
    float s=0; for(int d=0;d<16;d++) s+=qv[d]*b2f(qkv[k*1536+512+h*16+d]);
    float mn=fmaxf(m,s); float a=__expf(m-mn), p=__expf(s-mn); m=mn; l=l*a+p;
    for(int d=0;d<16;d++) o[d]=o[d]*a+p*b2f(qkv[k*1536+1024+h*16+d]);
  }
  for(int d=0;d<16;d++) O[q*512+h*16+d]=f2b(o[d]/l);
}
extern "C" void kernel_launch(void* const* d_in, const int* in_sizes, int n_in, void* d_out, int out_size, void* d_ws, size_t ws_size, hipStream_t stream){
  const u16* const* in=(const u16* const*)d_in;
  float* K=(float*)d_ws; u16* qkv=(u16*)((char*)d_ws+(8<<20)); u16* O=(u16*)((char*)d_ws+(16<<20));
  u16* out=(u16*)d_out;
  kgen<<<dim3(8,512),256,0,stream>>>(in[1],in[2],in[3],in[4],in[5],in[6],in[7],K);
  conv<<<dim3(8,512),256,0,stream>>>(in[0],K,in[8],out);
  gemm<<<dim3(24,512),256,0,stream>>>(in[9],in[10],qkv,1536,1536);
  attn<<<dim3(32,32),64,0,stream>>>(qkv,O);
  gemm<<<dim3(8,512),256,0,stream>>>(O,in[11],out+512,512,1024);
}
```

```cpp
#include <hip/hip_runtime.h>
typedef float f32x16 __attribute__((ext_vector_type(16)));
typedef float f32x4 __attribute__((ext_vector_type(4)));
typedef float f32x2 __attribute__((ext_vector_type(2)));
typedef __bf16 bf16x8 __attribute__((ext_vector_type(8)));
typedef __bf16 bf16x2 __attribute__((ext_vector_type(2)));
typedef short s16x4 __attribute__((ext_vector_type(4)));
typedef unsigned u32x4 __attribute__((ext_vector_type(4)));
typedef unsigned u32x2 __attribute__((ext_vector_type(2)));
typedef unsigned short u16;
typedef unsigned long long u64;
#define AS3 __attribute__((address_space(3)))
#define AS1 __attribute__((address_space(1)))
#define DEV __device__ __forceinline__

#define ATT_LOOP_ASM \
  "s_mov_b32 m0, s48\n\t" \
  "s_cmp_eq_u32 s44, 0\n\t" \
  "s_cbranch_scc1 2f\n\t" \
  "1:\n\t" \
  "v_exp_f32 v156, v156\n\t" \
  "v_exp_f32 v157, v157\n\t" \
  "v_smfmac_f32_32x32x32_bf16 v[140:155], v[232:235], v[180:187], v238\n\t" \
  "global_load_dwordx4 v[224:227], v236, s[40:41] offset:-4096 sc1\n\t" \
  "s_mov_b64 exec, s[42:43]\n\t" \
  "global_load_dwordx4 v[228:231], v237, s[40:41] offset:-3072 sc1\n\t" \
  "global_load_dwordx4 v[232:235], v237, s[40:41] offset:-2560 sc1\n\t" \
  "s_mov_b64 exec, -1\n\t" \
  "v_exp_f32 v158, v158\n\t" \
  "v_exp_f32 v159, v159\n\t" \
  "v_exp_f32 v160, v160\n\t" \
  "v_mfma_f32_32x32x16_bf16 v[172:187], v[188:191], v[104:107], v[108:123]\n\t" \
  "v_exp_f32 v161, v161\n\t" \
  "v_exp_f32 v162, v162\n\t" \
  "v_exp_f32 v163, v163\n\t" \
  "global_load_lds_dwordx4 v239, s[46:47]\n\t" \
  "v_exp_f32 v164, v164\n\t" \
  "s_add_u32 m0, m0, 1024\n\t" \
  "s_add_u32 s46, s46, 4096\n\t" \
  "s_addc_u32 s47, s47, 0\n\t" \
  "v_exp_f32 v165, v165\n\t" \
  "v_exp_f32 v166, v166\n\t" \
  "v_smfmac_f32_32x32x32_bf16 v[124:139], v[192:195], v[156:163], v238\n\t" \
  "v_exp_f32 v167, v167\n\t" \
  "v_exp_f32 v168, v168\n\t" \
  "v_exp_f32 v169, v169\n\t" \
  "v_exp_f32 v170, v170\n\t" \
  "v_exp_f32 v171, v171\n\t" \
  "v_exp_f32 v172, v172\n\t" \
  "v_exp_f32 v173, v173\n\t" \
  "v_smfmac_f32_32x32x32_bf16 v[124:139], v[196:199], v[164:171], v238\n\t" \
  "v_exp_f32 v174, v174\n\t" \
  "v_exp_f32 v175, v175\n\t" \
  "v_exp_f32 v176, v176\n\t" \
  "s_waitcnt vmcnt(9)\n\t" \
  "v_mfma_f32_32x32x16_bf16 v[156:171], v[200:203], v[100:103], v[108:123]\n\t" \
  "v_exp_f32 v177, v177\n\t" \
  "v_exp_f32 v178, v178\n\t" \
  "v_exp_f32 v179, v179\n\t" \
  "v_exp_f32 v180, v180\n\t" \
  "v_exp_f32 v181, v181\n\t" \
  "v_exp_f32 v182, v182\n\t" \
  "v_smfmac_f32_32x32x32_bf16 v[140:155], v[192:195], v[172:179], v238\n\t" \
  "v_exp_f32 v183, v183\n\t" \
  "v_exp_f32 v184, v184\n\t" \
  "v_exp_f32 v185, v185\n\t" \
  "v_exp_f32 v186, v186\n\t" \
  "v_exp_f32 v187, v187\n\t" \
  "v_exp_f32 v156, v156\n\t" \
  "v_exp_f32 v157, v157\n\t" \
  "v_smfmac_f32_32x32x32_bf16 v[140:155], v[196:199], v[180:187], v238\n\t" \
  "global_load_dwordx4 v[188:191], v236, s[40:41] offset:-2048 sc1\n\t" \
  "s_mov_b64 exec, s[42:43]\n\t" \
  "global_load_dwordx4 v[192:195], v237, s[40:41] offset:-1024 sc1\n\t" \
  "global_load_dwordx4 v[196:199], v237, s[40:41] offset:-512 sc1\n\t" \
  "s_mov_b64 exec, -1\n\t" \
  "v_exp_f32 v158, v158\n\t" \
  "v_exp_f32 v159, v159\n\t" \
  "v_exp_f32 v160, v160\n\t" \
  "v_mfma_f32_32x32x16_bf16 v[172:187], v[200:203], v[104:107], v[108:123]\n\t" \
  "v_exp_f32 v161, v161\n\t" \
  "v_exp_f32 v162, v162\n\t" \
  "v_exp_f32 v163, v163\n\t" \
  "global_load_lds_dwordx4 v239, s[46:47]\n\t" \
  "v_exp_f32 v164, v164\n\t" \
  "s_add_u32 m0, m0, 1024\n\t" \
  "s_add_u32 s46, s46, 4096\n\t" \
  "s_addc_u32 s47, s47, 0\n\t" \
  "v_exp_f32 v165, v165\n\t" \
  "v_exp_f32 v166, v166\n\t" \
  "v_smfmac_f32_32x32x32_bf16 v[124:139], v[204:207], v[156:163], v238\n\t" \
  "v_exp_f32 v167, v167\n\t" \
  "v_exp_f32 v168, v168\n\t" \
  "v_exp_f32 v169, v169\n\t" \
  "v_exp_f32 v170, v170\n\t" \
  "v_exp_f32 v171, v171\n\t" \
  "v_exp_f32 v172, v172\n\t" \
  "v_exp_f32 v173, v173\n\t" \
  "v_smfmac_f32_32x32x32_bf16 v[124:139], v[208:211], v[164:171], v238\n\t" \
  "v_exp_f32 v174, v174\n\t" \
  "v_exp_f32 v175, v175\n\t" \
  "v_exp_f32 v176, v176\n\t" \
  "s_waitcnt vmcnt(9)\n\t" \
  "v_mfma_f32_32x32x16_bf16 v[156:171], v[212:215], v[100:103], v[108:123]\n\t" \
  "v_exp_f32 v177, v177\n\t" \
  "v_exp_f32 v178, v178\n\t" \
  "v_exp_f32 v179, v179\n\t" \
  "v_exp_f32 v180, v180\n\t" \
  "v_exp_f32 v181, v181\n\t" \
  "v_exp_f32 v182, v182\n\t" \
  "v_smfmac_f32_32x32x32_bf16 v[140:155], v[204:207], v[172:179], v238\n\t" \
  "v_exp_f32 v183, v183\n\t" \
  "v_exp_f32 v184, v184\n\t" \
  "v_exp_f32 v185, v185\n\t" \
  "v_exp_f32 v186, v186\n\t" \
  "v_exp_f32 v187, v187\n\t" \
  "v_exp_f32 v156, v156\n\t" \
  "v_exp_f32 v157, v157\n\t" \
  "v_smfmac_f32_32x32x32_bf16 v[140:155], v[208:211], v[180:187], v238\n\t" \
  "global_load_dwordx4 v[200:203], v236, s[40:41] offset:0 sc1\n\t" \
  "s_mov_b64 exec, s[42:43]\n\t" \
  "global_load_dwordx4 v[204:207], v237, s[40:41] offset:1024 sc1\n\t" \
  "global_load_dwordx4 v[208:211], v237, s[40:41] offset:1536 sc1\n\t" \
  "s_mov_b64 exec, -1\n\t" \
  "v_exp_f32 v158, v158\n\t" \
  "v_exp_f32 v159, v159\n\t" \
  "v_exp_f32 v160, v160\n\t" \
  "v_mfma_f32_32x32x16_bf16 v[172:187], v[212:215], v[104:107], v[108:123]\n\t" \
  "v_exp_f32 v161, v161\n\t" \
  "v_exp_f32 v162, v162\n\t" \
  "v_exp_f32 v163, v163\n\t" \
  "global_load_lds_dwordx4 v239, s[46:47]\n\t" \
  "v_exp_f32 v164, v164\n\t" \
  "s_add_u32 m0, m0, 1024\n\t" \
  "s_add_u32 s46, s46, 4096\n\t" \
  "s_addc_u32 s47, s47, 0\n\t" \
  "v_exp_f32 v165, v165\n\t" \
  "v_exp_f32 v166, v166\n\t" \
  "v_smfmac_f32_32x32x32_bf16 v[124:139], v[216:219], v[156:163], v238\n\t" \
  "v_exp_f32 v167, v167\n\t" \
  "v_exp_f32 v168, v168\n\t" \
  "v_exp_f32 v169, v169\n\t" \
  "v_exp_f32 v170, v170\n\t" \
  "v_exp_f32 v171, v171\n\t" \
  "v_exp_f32 v172, v172\n\t" \
  "v_exp_f32 v173, v173\n\t" \
  "v_smfmac_f32_32x32x32_bf16 v[124:139], v[220:223], v[164:171], v238\n\t" \
  "v_exp_f32 v174, v174\n\t" \
  "v_exp_f32 v175, v175\n\t" \
  "v_exp_f32 v176, v176\n\t" \
  "s_waitcnt vmcnt(9)\n\t" \
  "v_mfma_f32_32x32x16_bf16 v[156:171], v[224:227], v[100:103], v[108:123]\n\t" \
  "v_exp_f32 v177, v177\n\t" \
  "v_exp_f32 v178, v178\n\t" \
  "v_exp_f32 v179, v179\n\t" \
  "v_exp_f32 v180, v180\n\t" \
  "v_exp_f32 v181, v181\n\t" \
  "v_exp_f32 v182, v182\n\t" \
  "v_smfmac_f32_32x32x32_bf16 v[140:155], v[216:219], v[172:179], v238\n\t" \
  "v_exp_f32 v183, v183\n\t" \
  "v_exp_f32 v184, v184\n\t" \
  "v_exp_f32 v185, v185\n\t" \
  "v_exp_f32 v186, v186\n\t" \
  "v_exp_f32 v187, v187\n\t" \
  "v_exp_f32 v156, v156\n\t" \
  "v_exp_f32 v157, v157\n\t" \
  "v_smfmac_f32_32x32x32_bf16 v[140:155], v[220:223], v[180:187], v238\n\t" \
  "global_load_dwordx4 v[212:215], v236, s[40:41] offset:2048 sc1\n\t" \
  "s_mov_b64 exec, s[42:43]\n\t" \
  "global_load_dwordx4 v[216:219], v237, s[40:41] offset:3072 sc1\n\t" \
  "global_load_dwordx4 v[220:223], v237, s[40:41] offset:3584 sc1\n\t" \
  "s_mov_b64 exec, -1\n\t" \
  "v_exp_f32 v158, v158\n\t" \
  "v_exp_f32 v159, v159\n\t" \
  "v_exp_f32 v160, v160\n\t" \
  "v_mfma_f32_32x32x16_bf16 v[172:187], v[224:227], v[104:107], v[108:123]\n\t" \
  "v_exp_f32 v161, v161\n\t" \
  "v_exp_f32 v162, v162\n\t" \
  "v_exp_f32 v163, v163\n\t" \
  "global_load_lds_dwordx4 v239, s[46:47]\n\t" \
  "v_exp_f32 v164, v164\n\t" \
  "s_add_u32 m0, m0, 1024\n\t" \
  "s_add_u32 s46, s46, 4096\n\t" \
  "s_addc_u32 s47, s47, 0\n\t" \
  "v_exp_f32 v165, v165\n\t" \
  "v_exp_f32 v166, v166\n\t" \
  "v_smfmac_f32_32x32x32_bf16 v[124:139], v[228:231], v[156:163], v238\n\t" \
  "v_exp_f32 v167, v167\n\t" \
  "v_exp_f32 v168, v168\n\t" \
  "v_exp_f32 v169, v169\n\t" \
  "v_exp_f32 v170, v170\n\t" \
  "v_exp_f32 v171, v171\n\t" \
  "v_exp_f32 v172, v172\n\t" \
  "v_exp_f32 v173, v173\n\t" \
  "v_smfmac_f32_32x32x32_bf16 v[124:139], v[232:235], v[164:171], v238\n\t" \
  "v_exp_f32 v174, v174\n\t" \
  "v_exp_f32 v175, v175\n\t" \
  "v_exp_f32 v176, v176\n\t" \
  "s_waitcnt vmcnt(9)\n\t" \
  "v_mfma_f32_32x32x16_bf16 v[156:171], v[188:191], v[100:103], v[108:123]\n\t" \
  "v_exp_f32 v177, v177\n\t" \
  "v_exp_f32 v178, v178\n\t" \
  "v_exp_f32 v179, v179\n\t" \
  "v_exp_f32 v180, v180\n\t" \
  "v_exp_f32 v181, v181\n\t" \
  "v_exp_f32 v182, v182\n\t" \
  "v_smfmac_f32_32x32x32_bf16 v[140:155], v[228:231], v[172:179], v238\n\t" \
  "v_exp_f32 v183, v183\n\t" \
  "v_exp_f32 v184, v184\n\t" \
  "v_exp_f32 v185, v185\n\t" \
  "v_exp_f32 v186, v186\n\t" \
  "v_exp_f32 v187, v187\n\t" \
  "s_add_u32 s40, s40, 8192\n\t" \
  "s_addc_u32 s41, s41, 0\n\t" \
  "s_sub_u32 s44, s44, 1\n\t" \
  "s_cmp_lg_u32 s44, 0\n\t" \
  "s_cbranch_scc1 1b\n\t" \
  "2:\n\t" \
  "v_exp_f32 v156, v156\n\t" \
  "v_exp_f32 v157, v157\n\t" \
  "v_smfmac_f32_32x32x32_bf16 v[140:155], v[232:235], v[180:187], v238\n\t" \
  "global_load_dwordx4 v[224:227], v236, s[40:41] offset:-4096 sc1\n\t" \
  "s_mov_b64 exec, s[42:43]\n\t" \
  "global_load_dwordx4 v[228:231], v237, s[40:41] offset:-3072 sc1\n\t" \
  "global_load_dwordx4 v[232:235], v237, s[40:41] offset:-2560 sc1\n\t" \
  "s_mov_b64 exec, -1\n\t" \
  "v_exp_f32 v158, v158\n\t" \
  "v_exp_f32 v159, v159\n\t" \
  "v_exp_f32 v160, v160\n\t" \
  "v_mfma_f32_32x32x16_bf16 v[172:187], v[188:191], v[104:107], v[108:123]\n\t" \
  "v_exp_f32 v161, v161\n\t" \
  "v_exp_f32 v162, v162\n\t" \
  "v_exp_f32 v163, v163\n\t" \
  "v_exp_f32 v164, v164\n\t" \
  "v_exp_f32 v165, v165\n\t" \
  "v_exp_f32 v166, v166\n\t" \
  "v_smfmac_f32_32x32x32_bf16 v[124:139], v[192:195], v[156:163], v238\n\t" \
  "v_exp_f32 v167, v167\n\t" \
  "v_exp_f32 v168, v168\n\t" \
  "v_exp_f32 v169, v169\n\t" \
  "v_exp_f32 v170, v170\n\t" \
  "v_exp_f32 v171, v171\n\t" \
  "v_exp_f32 v172, v172\n\t" \
  "v_exp_f32 v173, v173\n\t" \
  "v_smfmac_f32_32x32x32_bf16 v[124:139], v[196:199], v[164:171], v238\n\t" \
  "v_exp_f32 v174, v174\n\t" \
  "v_exp_f32 v175, v175\n\t" \
  "v_exp_f32 v176, v176\n\t" \
  "s_waitcnt vmcnt(6)\n\t" \
  "v_mfma_f32_32x32x16_bf16 v[156:171], v[200:203], v[100:103], v[108:123]\n\t" \
  "v_exp_f32 v177, v177\n\t" \
  "v_exp_f32 v178, v178\n\t" \
  "v_exp_f32 v179, v179\n\t" \
  "v_exp_f32 v180, v180\n\t" \
  "v_exp_f32 v181, v181\n\t" \
  "v_exp_f32 v182, v182\n\t" \
  "v_smfmac_f32_32x32x32_bf16 v[140:155], v[192:195], v[172:179], v238\n\t" \
  "v_exp_f32 v183, v183\n\t" \
  "v_exp_f32 v184, v184\n\t" \
  "v_exp_f32 v185, v185\n\t" \
  "v_exp_f32 v186, v186\n\t" \
  "v_exp_f32 v187, v187\n\t" \
  "v_exp_f32 v156, v156\n\t" \
  "v_exp_f32 v157, v157\n\t" \
  "v_smfmac_f32_32x32x32_bf16 v[140:155], v[196:199], v[180:187], v238\n\t" \
  "global_load_dwordx4 v[188:191], v236, s[40:41] offset:-2048 sc1\n\t" \
  "s_mov_b64 exec, s[42:43]\n\t" \
  "global_load_dwordx4 v[192:195], v237, s[40:41] offset:-1024 sc1\n\t" \
  "global_load_dwordx4 v[196:199], v237, s[40:41] offset:-512 sc1\n\t" \
  "s_mov_b64 exec, -1\n\t" \
  "v_exp_f32 v158, v158\n\t" \
  "v_exp_f32 v159, v159\n\t" \
  "v_exp_f32 v160, v160\n\t" \
  "v_mfma_f32_32x32x16_bf16 v[172:187], v[200:203], v[104:107], v[108:123]\n\t" \
  "v_exp_f32 v161, v161\n\t" \
  "v_exp_f32 v162, v162\n\t" \
  "v_exp_f32 v163, v163\n\t" \
  "v_exp_f32 v164, v164\n\t" \
  "v_exp_f32 v165, v165\n\t" \
  "v_exp_f32 v166, v166\n\t" \
  "v_smfmac_f32_32x32x32_bf16 v[124:139], v[204:207], v[156:163], v238\n\t" \
  "v_exp_f32 v167, v167\n\t" \
  "v_exp_f32 v168, v168\n\t" \
  "v_exp_f32 v169, v169\n\t" \
  "v_exp_f32 v170, v170\n\t" \
  "v_exp_f32 v171, v171\n\t" \
  "v_exp_f32 v172, v172\n\t" \
  "v_exp_f32 v173, v173\n\t" \
  "v_smfmac_f32_32x32x32_bf16 v[124:139], v[208:211], v[164:171], v238\n\t" \
  "v_exp_f32 v174, v174\n\t" \
  "v_exp_f32 v175, v175\n\t" \
  "v_exp_f32 v176, v176\n\t" \
  "s_waitcnt vmcnt(6)\n\t" \
  "v_mfma_f32_32x32x16_bf16 v[156:171], v[212:215], v[100:103], v[108:123]\n\t" \
  "v_exp_f32 v177, v177\n\t" \
  "v_exp_f32 v178, v178\n\t" \
  "v_exp_f32 v179, v179\n\t" \
  "v_exp_f32 v180, v180\n\t" \
  "v_exp_f32 v181, v181\n\t" \
  "v_exp_f32 v182, v182\n\t" \
  "v_smfmac_f32_32x32x32_bf16 v[140:155], v[204:207], v[172:179], v238\n\t" \
  "v_exp_f32 v183, v183\n\t" \
  "v_exp_f32 v184, v184\n\t" \
  "v_exp_f32 v185, v185\n\t" \
  "v_exp_f32 v186, v186\n\t" \
  "v_exp_f32 v187, v187\n\t" \
  "v_exp_f32 v156, v156\n\t" \
  "v_exp_f32 v157, v157\n\t" \
  "v_smfmac_f32_32x32x32_bf16 v[140:155], v[208:211], v[180:187], v238\n\t" \
  "global_load_dwordx4 v[200:203], v236, s[40:41] offset:0 sc1\n\t" \
  "s_mov_b64 exec, s[42:43]\n\t" \
  "global_load_dwordx4 v[204:207], v237, s[40:41] offset:1024 sc1\n\t" \
  "global_load_dwordx4 v[208:211], v237, s[40:41] offset:1536 sc1\n\t" \
  "s_mov_b64 exec, -1\n\t" \
  "v_exp_f32 v158, v158\n\t" \
  "v_exp_f32 v159, v159\n\t" \
  "v_exp_f32 v160, v160\n\t" \
  "v_mfma_f32_32x32x16_bf16 v[172:187], v[212:215], v[104:107], v[108:123]\n\t" \
  "v_exp_f32 v161, v161\n\t" \
  "v_exp_f32 v162, v162\n\t" \
  "v_exp_f32 v163, v163\n\t" \
  "v_exp_f32 v164, v164\n\t" \
  "v_exp_f32 v165, v165\n\t" \
  "v_exp_f32 v166, v166\n\t" \
  "v_smfmac_f32_32x32x32_bf16 v[124:139], v[216:219], v[156:163], v238\n\t" \
  "v_exp_f32 v167, v167\n\t" \
  "v_exp_f32 v168, v168\n\t" \
  "v_exp_f32 v169, v169\n\t" \
  "v_exp_f32 v170, v170\n\t" \
  "v_exp_f32 v171, v171\n\t" \
  "v_exp_f32 v172, v172\n\t" \
  "v_exp_f32 v173, v173\n\t" \
  "v_smfmac_f32_32x32x32_bf16 v[124:139], v[220:223], v[164:171], v238\n\t" \
  "v_exp_f32 v174, v174\n\t" \
  "v_exp_f32 v175, v175\n\t" \
  "v_exp_f32 v176, v176\n\t" \
  "s_waitcnt vmcnt(6)\n\t" \
  "v_mfma_f32_32x32x16_bf16 v[156:171], v[224:227], v[100:103], v[108:123]\n\t" \
  "v_exp_f32 v177, v177\n\t" \
  "v_exp_f32 v178, v178\n\t" \
  "v_exp_f32 v179, v179\n\t" \
  "v_exp_f32 v180, v180\n\t" \
  "v_exp_f32 v181, v181\n\t" \
  "v_exp_f32 v182, v182\n\t" \
  "v_smfmac_f32_32x32x32_bf16 v[140:155], v[216:219], v[172:179], v238\n\t" \
  "v_exp_f32 v183, v183\n\t" \
  "v_exp_f32 v184, v184\n\t" \
  "v_exp_f32 v185, v185\n\t" \
  "v_exp_f32 v186, v186\n\t" \
  "v_exp_f32 v187, v187\n\t" \
  "v_exp_f32 v156, v156\n\t" \
  "v_exp_f32 v157, v157\n\t" \
  "v_smfmac_f32_32x32x32_bf16 v[140:155], v[220:223], v[180:187], v238\n\t" \
  "global_load_dwordx4 v[212:215], v236, s[40:41] offset:2048 sc1\n\t" \
  "s_mov_b64 exec, s[42:43]\n\t" \
  "global_load_dwordx4 v[216:219], v237, s[40:41] offset:3072 sc1\n\t" \
  "global_load_dwordx4 v[220:223], v237, s[40:41] offset:3584 sc1\n\t" \
  "s_mov_b64 exec, -1\n\t" \
  "v_exp_f32 v158, v158\n\t" \
  "v_exp_f32 v159, v159\n\t" \
  "v_exp_f32 v160, v160\n\t" \
  "v_mfma_f32_32x32x16_bf16 v[172:187], v[224:227], v[104:107], v[108:123]\n\t" \
  "v_exp_f32 v161, v161\n\t" \
  "v_exp_f32 v162, v162\n\t" \
  "v_exp_f32 v163, v163\n\t" \
  "v_exp_f32 v164, v164\n\t" \
  "v_exp_f32 v165, v165\n\t" \
  "v_exp_f32 v166, v166\n\t" \
  "v_smfmac_f32_32x32x32_bf16 v[124:139], v[228:231], v[156:163], v238\n\t" \
  "v_exp_f32 v167, v167\n\t" \
  "v_exp_f32 v168, v168\n\t" \
  "v_exp_f32 v169, v169\n\t" \
  "v_exp_f32 v170, v170\n\t" \
  "v_exp_f32 v171, v171\n\t" \
  "v_exp_f32 v172, v172\n\t" \
  "v_exp_f32 v173, v173\n\t" \
  "v_smfmac_f32_32x32x32_bf16 v[124:139], v[232:235], v[164:171], v238\n\t" \
  "v_exp_f32 v174, v174\n\t" \
  "v_exp_f32 v175, v175\n\t" \
  "v_exp_f32 v176, v176\n\t" \
  "s_waitcnt vmcnt(6)\n\t" \
  "v_mfma_f32_32x32x16_bf16 v[156:171], v[188:191], v[100:103], v[108:123]\n\t" \
  "v_exp_f32 v177, v177\n\t" \
  "v_exp_f32 v178, v178\n\t" \
  "v_exp_f32 v179, v179\n\t" \
  "v_exp_f32 v180, v180\n\t" \
  "v_exp_f32 v181, v181\n\t" \
  "v_exp_f32 v182, v182\n\t" \
  "v_smfmac_f32_32x32x32_bf16 v[140:155], v[228:231], v[172:179], v238\n\t" \
  "v_exp_f32 v183, v183\n\t" \
  "v_exp_f32 v184, v184\n\t" \
  "v_exp_f32 v185, v185\n\t" \
  "v_exp_f32 v186, v186\n\t" \
  "v_exp_f32 v187, v187\n\t" \
  "s_add_u32 s40, s40, 8192\n\t" \
  "s_addc_u32 s41, s41, 0\n\t" \
  "s_sub_u32 s45, s45, 1\n\t" \
  "s_cmp_lg_u32 s45, 0\n\t" \
  "s_cbranch_scc1 2b\n\t" \
  "s_nop 1\n\t" \
  "v_smfmac_f32_32x32x32_bf16 v[140:155], v[232:235], v[180:187], v238\n\t" \
  "s_waitcnt vmcnt(0)\n\t" \
  "s_nop 15\n\t"

#define NA_STEPS 36
#define KG_IN 4

constexpr unsigned WS_F1 = 4096;
constexpr unsigned WS_F2 = WS_F1 + 32768;
constexpr unsigned WS_DBG = 512u << 10;
constexpr unsigned KV_HEAD = 68 * 2048;
constexpr unsigned WS_KV = 1u << 20;
constexpr unsigned WS_UT = 6u << 20;
constexpr unsigned WS_YT = 8u << 20;
constexpr unsigned WS_O = 10u << 20;
constexpr unsigned WS_END = 12u << 20;
constexpr unsigned L_X = 0, L_R = 65536, L_S = 131072;
constexpr unsigned L_UT = L_S, L_PR = L_S + 8192, L_KF = L_S + 12288;
constexpr unsigned L_EX = L_KF + 8320;
constexpr unsigned L_OV = L_EX + 10240;
constexpr unsigned L_FL = L_OV + 288;
constexpr unsigned L_QF = L_R + 32768;
constexpr unsigned L_END = 163840;

DEV float b2f(unsigned x) { return __uint_as_float(x << 16); }
DEV unsigned pk(float a, float b) { f32x2 v = {a, b}; return __builtin_bit_cast(unsigned, __builtin_convertvector(v, bf16x2)); }
DEV f32x16 mfma(u32x4 a, u32x4 b, f32x16 c) {
  return __builtin_amdgcn_mfma_f32_32x32x16_bf16(__builtin_bit_cast(bf16x8, a), __builtin_bit_cast(bf16x8, b), c, 0, 0, 0);
}
typedef AS3 char* lds_t;
DEV lds_t lb(unsigned off) { asm("" : "+v"(off)); return (lds_t)(size_t)off; }
template <class T> DEV AS3 T* lp(unsigned off) { return (AS3 T*)(size_t)(off); }
template <class T> DEV AS3 T* lp(lds_t p) { return (AS3 T*)p; }
DEV u32x2 trd(lds_t p) { return __builtin_bit_cast(u32x2, __builtin_amdgcn_ds_read_tr16_b64_v4i16((AS3 s16x4*)p)); }
DEV u32x4 trd2(lds_t a, lds_t b) { u32x2 x = trd(a), y = trd(b); return u32x4{x[0], x[1], y[0], y[1]}; }
DEV void dma(const char* sbase, unsigned voff, unsigned dst) {
  asm volatile("s_mov_b32 m0, %2\n\ts_nop 0\n\tglobal_load_lds_dwordx4 %0, %1" ::"v"(voff), "s"(sbase), "s"(dst) : "memory");
}
#define BAR() asm volatile("s_waitcnt lgkmcnt(0)\n\ts_barrier" ::: "memory")
#define VMW(n) asm volatile("s_waitcnt vmcnt(" #n ")" ::: "memory")
#define VMBAR(n) asm volatile("s_waitcnt vmcnt(" #n ") lgkmcnt(0)\n\ts_barrier" ::: "memory")
#define CFENCE() asm volatile("" ::: "memory")
#define SC1 16
#define ST(i)
constexpr unsigned SPIN_CAP = 1u << 16;

DEV void poll(const char* p, u64 tag) {
#pragma unroll 1
  for (unsigned s = 0; s < SPIN_CAP; ++s) {
    u64 v = __hip_atomic_load((const AS1 u64*)p, __ATOMIC_RELAXED, __HIP_MEMORY_SCOPE_AGENT);
    if (__all(v == tag)) break;
    __builtin_amdgcn_s_sleep(1);
  }
  CFENCE();
}

DEV u32x4 kg_side(lds_t pr, float a, bool sideA) {
  u32x4 o;
#pragma unroll
  for (int e = 0; e < 4; ++e) {
    f32x4 p = *lp<f32x4>(pr + e * 16);
    float ex = sideA ? __builtin_fmaf(p[0], a, p[2]) : p[0] * a;
    float th = sideA ? __builtin_fmaf(p[1], a, p[3]) : p[1] * a;
    th = __builtin_amdgcn_fractf(th);
    float m = __builtin_amdgcn_exp2f(ex);
    float c = __builtin_amdgcn_cosf(th), s = __builtin_amdgcn_sinf(th);
    o[e] = pk(m * c, sideA ? -m * s : m * s);
  }
  return o;
}

template <int SET> DEV void conv_wave(unsigned fa, unsigned ga, f32x16 (&acc)[4]) {
  constexpr int T[4] = {SET ? 6 : 7, SET ? 5 : 4, SET ? 2 : 3, SET ? 1 : 0};
#pragma unroll 1
  for (int rho = 0; rho < 4; ++rho) {
    u32x4 F[8], G[8];
    lds_t f = lb(fa), g = lb(ga);
#pragma unroll
    for (int a = 0; a <= T[0]; ++a) {
      F[a] = trd2(f + 512 * a + 32, f + 512 * a);
      G[T[0] - a] = *lp<u32x4>(g + 512 * (T[0] - a));
    }
    __builtin_amdgcn_sched_barrier(0);
#pragma unroll
    for (int k = 0; k < 4; ++k)
#pragma unroll
      for (int a = 0; a <= T[k]; ++a) acc[k] = mfma(G[T[k] - a], F[a], acc[k]);
    fa += 128; ga -= 128;
  }
}

__global__ void __launch_bounds__(512, 2)
fused(const char* __restrict__ u, const u16* __restrict__ Are, const u16* __restrict__ Aim, const u16* __restrict__ Bre,
      const u16* __restrict__ Bim, const u16* __restrict__ Cre, const u16* __restrict__ Cim, const u16* __restrict__ ldt,
      const char* __restrict__ Dsk, const char* __restrict__ x, const char* __restrict__ wqkv, const char* __restrict__ wo,
      char* __restrict__ out, char* __restrict__ ws) {
  __shared__ __attribute__((aligned(16))) char lds_all[L_END];
  asm volatile("" ::"v"(lds_all));
  const int b = blockIdx.x, hq = b & 7, qb = b >> 3;
  const int lane = threadIdx.x & 63, w = __builtin_amdgcn_readfirstlane(threadIdx.x >> 6);
  const int r = lane & 31, h = lane >> 5;
  const int q4 = (lane >> 2) & 3, pp = lane & 3, g1 = (lane >> 4) & 1;
  const int p = w & 1, t = (w >> 1) & 1;
  const __amdgpu_buffer_rsrc_t wsr = __builtin_amdgcn_make_buffer_rsrc(ws, 0, WS_END, 0x00020000);
  ST(0);
  const unsigned wt0 = (8 * h + q4) * 128 + ((64 * p + 32 * g1 + 8 * pp) ^ (64 * (q4 >> 1)));
  const lds_t wta = lb(L_R + wt0);
  u64 tag;
  unsigned epoch;
  u32x4 q0, q1;

  if (w >= 4) {
    const int kw = w - 4, rot = qb & 7;
    unsigned e0;
    asm volatile("global_load_dword %0, %1, %2 sc1" : "=v"(e0) : "v"(0u), "s"(ws) : "memory");
    const unsigned row = 16 * kw + (lane >> 3);
    const unsigned xo0 = row * 1024 + (((lane & 7) ^ (lane >> 4)) << 4);
    const unsigned xo1 = (row + 8) * 1024 + (((lane & 7) ^ (lane >> 4) ^ 4) << 4);
    const unsigned wo0 = row * 3072 + (((lane & 7) ^ ((lane >> 2) & 4)) << 4);
    const unsigned wo1 = wo0 + 8 * 3072;
    const char* xb = x + qb * 65536;
    const char* wb = wqkv + hq * 128;
    const unsigned ld = kw * 2048;
    auto DX = [&](int c) { const char* s = xb + (((c + rot) & 7) << 7); dma(s, xo0, L_X + c * 8192 + ld); dma(s, xo1, L_X + c * 8192 + ld + 1024); };
    auto DW = [&](int c, int seg, unsigned dst) { const char* s = wb + ((c + rot) & 7) * (64 * 3072) + seg * 1024; dma(s, wo0, dst + ld); dma(s, wo1, dst + ld + 1024); };
    auto DKV = [&](int c) { DW(c, 1, L_R + (c & 3) * 16384); DW(c, 2, L_R + (c & 3) * 16384 + 8192); };
    constexpr unsigned QL[8] = {L_X, L_X + 8192, L_X + 16384, L_X + 24576, L_X + 32768, L_R, L_R + 8192, L_X + 40960};
#pragma unroll
    for (int c = 0; c < 4; ++c) { DX(c); DKV(c); }
#pragma unroll
    for (int c = 4; c < 8; ++c) DX(c);
    ST(1);
    const unsigned xrow = 32 * t + r;
    const unsigned xa = L_X + xrow * 128 + ((h ^ ((xrow >> 1) & 7)) << 4);
    const lds_t xs[4] = {lb(xa), lb(xa ^ 32), lb(xa ^ 64), lb(xa ^ 96)};
    u32x4 xr[32];
    f32x16 acc = {0};
#pragma unroll
    for (int c = 0; c < 8; ++c) {
      if (c == 0) VMBAR(26);
      if (c == 1) { VMBAR(20); DKV(4); }
      if (c == 2) { VMBAR(18); DKV(5); }
      if (c == 3) { VMBAR(16); DKV(6); }
      if (c == 4) { VMBAR(8); DKV(7); DW(0, 0, QL[0]); DW(1, 0, QL[1]); DW(2, 0, QL[2]); DW(3, 0, QL[3]); }
      if (c == 5) { VMBAR(16); DW(4, 0, QL[4]); DW(5, 0, QL[5]); DW(6, 0, QL[6]); }
      if (c == 6) { VMBAR(18); DW(7, 0, QL[7]); }
      if (c == 7) VMBAR(16);
#pragma unroll
      for (int s = 0; s < 4; ++s) {
        xr[4 * c + s] = *lp<u32x4>(xs[s] + c * 8192);
        lds_t a = wta + (c & 3) * 16384 + s * 2048;
        acc = mfma(trd2(a, a + 512), xr[4 * c + s], acc);
      }
    }
    ST(2);
    {
      unsigned vo = WS_KV + (4 * hq + 2 * p) * KV_HEAD + (2 * qb + t) * 2048 + lane * 16;
      __builtin_amdgcn_raw_buffer_store_b128(u32x4{pk(acc[0], acc[1]), pk(acc[2], acc[3]), pk(acc[4], acc[5]), pk(acc[6], acc[7])}, wsr, vo, 0, SC1);
      __builtin_amdgcn_raw_buffer_store_b128(u32x4{pk(acc[8], acc[9]), pk(acc[10], acc[11]), pk(acc[12], acc[13]), pk(acc[14], acc[15])}, wsr, vo + KV_HEAD, 0, SC1);
    }
    asm volatile("s_waitcnt vmcnt(0) lgkmcnt(0)\n\ts_barrier" : "+v"(e0)::"memory");
    epoch = __builtin_amdgcn_readfirstlane(e0) * 1664525u + 1013904223u;
    tag = ((u64)(epoch * 0x9E3779B1u + 0x7F4A7C15u) << 32) | epoch;
    if (w == 4 && lane == 0) __hip_atomic_store((AS1 u64*)(ws + WS_F1 + (hq * 32 + qb) * 128), tag, __ATOMIC_RELAXED, __HIP_MEMORY_SCOPE_AGENT);
    ST(3);
    acc = f32x16{0};
    const lds_t wtx = lb(wt0);
#pragma unroll
    for (int c = 0; c < 8; ++c)
#pragma unroll
      for (int s = 0; s < 4; ++s) {
        lds_t a = (QL[c] >= L_R ? wta + (QL[c] - L_R) : wtx + QL[c]) + s * 2048;
        acc = mfma(trd2(a, a + 512), xr[4 * c + s], acc);
      }
    const float qs = 0.25f * 1.44269504088896341f;
    acc *= qs;
    lds_t qa = lb(L_QF + (2 * p) * 2048 + t * 1024 + lane * 16);
    *lp<u32x4>(qa) = u32x4{pk(acc[0], acc[1]), pk(acc[2], acc[3]), pk(acc[4], acc[5]), pk(acc[6], acc[7])};
    *lp<u32x4>(qa + 2048) = u32x4{pk(acc[8], acc[9]), pk(acc[10], acc[11]), pk(acc[12], acc[13]), pk(acc[14], acc[15])};
    ST(4);
    BAR();
    q0 = *lp<u32x4>(L_QF + (w & 3) * 2048 + lane * 16);
    q1 = *lp<u32x4>(L_QF + (w & 3) * 2048 + 1024 + lane * 16);
    BAR();
    ST(5);
    poll(ws + WS_F1 + (hq * 32 + r) * 128, tag);
    __builtin_amdgcn_s_setprio(1);
  } else {
    const int cl = w >> 1, mh = w & 1, ch = hq * 64 + 2 * qb + cl;
    const unsigned pi = ch * 128 + mh * 64 + lane;
    unsigned e0 = __hip_atomic_load((const AS1 unsigned*)ws, __ATOMIC_RELAXED, __HIP_MEMORY_SCOPE_AGENT);
    const char* ub = u + (qb * 64 + 4 * (lane >> 3) + w) * 1024 + hq * 128 + (lane & 7) * 16;
    u32x4 ua = *(const u32x4*)ub, uc = *(const u32x4*)(ub + 32 * 1024);
    float dt = b2f(ldt[ch]);
    float ar = b2f(Are[pi]), ai = b2f(Aim[pi]), br = b2f(Bre[pi]), bi = b2f(Bim[pi]), cr = b2f(Cre[pi]), ci = b2f(Cim[pi]);
    {
      unsigned vo = WS_O + (2 * qb * 32 + 4 * hq + w) * 1024 + lane * 16;
      u32x4 ones = {~0u, ~0u, ~0u, ~0u};
      __builtin_amdgcn_raw_buffer_store_b128(ones, wsr, vo, 0, SC1);
      __builtin_amdgcn_raw_buffer_store_b128(ones, wsr, vo + 32768, 0, SC1);
    }
    if (w == 0 && lane < 16) *lp<unsigned>(L_FL + lane * 4) = 0;
    if (lane < 4) { *lp<u32x4>(L_KF + cl * 4160 + lane * 16) = u32x4{0}; *lp<unsigned>(L_OV + cl * 144 + lane * 4) = 0; }
    epoch = __builtin_amdgcn_readfirstlane(e0) * 1664525u + 1013904223u;
    tag = ((u64)(epoch * 0x9E3779B1u + 0x7F4A7C15u) << 32) | epoch;
    {
      unsigned ul = L_UT + w * 2048;
      *lp<u32x4>(ul + lane * 16) = ua;
      *lp<u32x4>(ul + 1024 + lane * 16) = uc;
      lds_t a = lb(ul + q4 * 128 + (lane >> 4) * 32 + pp * 8);
      u32x4 t0 = trd2(a, a + 512), t1 = trd2(a + 1024, a + 1536);
      unsigned vo = WS_UT + ((hq * 32 + qb) * 8 + 2 * w) * 1024 + lane * 16;
      __builtin_amdgcn_raw_buffer_store_b128(t0, wsr, vo, 0, SC1);
      __builtin_amdgcn_raw_buffer_store_b128(t1, wsr, vo + 1024, 0, SC1);
    }
    {
      dt = b2f(pk(__expf(dt), 0.f));
      br *= dt; bi *= dt;
      float wr = cr * br - ci * bi, wi = cr * bi + ci * br;
      float mx = fmaxf(fabsf(wr), fabsf(wi)), mn = fminf(fabsf(wr), fabsf(wi));
      float rt = mx > 0.f ? mn / mx : 0.f;
      f32x4 pv;
      pv[0] = fmaxf(dt * ar * 1.44269504088896341f, -1e30f);
      pv[1] = dt * ai * 0.15915494309189535f;
      pv[2] = __log2f(mx) + 0.5f * __log2f(1.f + rt * rt);
      pv[3] = atan2f(wi, wr) * 0.15915494309189535f;
      *lp<f32x4>(L_PR + w * 1024 + lane * 16) = pv;
    }
    float aA, aB = (float)(8 * r);
    {
      int e = r & 3, g = r >> 2;
      int v = g < 2 ? r : g < 4 ? 256 + 4 * (g - 2) + (e ? 2 * (e - 1) : 0) : g < 6 ? 512 + 256 * (g - 4) + 4 * (e < 2 ? e : 2) : 1024 + 512 * (g - 6) + 256 * (e >> 1) + 8 * (e & 1);
      aA = (float)v;
    }
    const lds_t pr = lb(L_PR + w * 1024 + h * 64);
    ST(1);
    const int rs = (r & 19) | ((r & 4) << 1) | ((r & 8) >> 1);
    const unsigned xrow = 32 * t + rs;
    const unsigned xa = L_X + xrow * 128 + ((h ^ ((xrow >> 1) & 7)) << 4);
    const lds_t xs[4] = {lb(xa), lb(xa ^ 32), lb(xa ^ 64), lb(xa ^ 96)};
    f32x16 acc = {0}, kacc = {0};
    u32x4 fa;
#pragma unroll
    for (int c = 0; c < 8; ++c) {
      BAR();
      u32x4 xf[4], wf[4];
#pragma unroll
      for (int s = 0; s < 4; ++s) {
        xf[s] = *lp<u32x4>(xs[s] + c * 8192);
        lds_t a = wta + (c & 3) * 16384 + 8192 + s * 2048;
        wf[s] = trd2(a, a + 512);
      }
      if ((c >> 1) < KG_IN) {
        if (c & 1) kacc = mfma(fa, kg_side(pr + (c >> 1) * 128, aB, false), kacc);
        else fa = kg_side(pr + (c >> 1) * 128, aA, true);
      }
#pragma unroll
      for (int s = 0; s < 4; ++s) acc = mfma(xf[s], wf[s], acc);
    }
    ST(2);
    {
      unsigned vo = WS_KV + (4 * hq + 2 * p + g1) * KV_HEAD + (2 * qb + t) * 2048 + 1024 + h * 256 + (lane & 15) * 16;
      __builtin_amdgcn_raw_buffer_store_b128(u32x4{pk(acc[0], acc[1]), pk(acc[2], acc[3]), pk(acc[4], acc[5]), pk(acc[6], acc[7])}, wsr, vo, 0, SC1);
      __builtin_amdgcn_raw_buffer_store_b128(u32x4{pk(acc[8], acc[9]), pk(acc[10], acc[11]), pk(acc[12], acc[13]), pk(acc[14], acc[15])}, wsr, vo + 512, 0, SC1);
    }
    CFENCE();
#pragma unroll 1
    for (int s = KG_IN; s < 8; ++s) {
      kacc = mfma(kg_side(pr + s * 128, aA, true), kg_side(pr + s * 128, aB, false), kacc);
      if (s == (KG_IN < 7 ? KG_IN + 1 : 7)) { VMBAR(0); ST(3); }
    }
    {
      lds_t pa = lb(L_UT + w * 2048 + lane * 16);
      if (mh) { *lp<f32x4>(pa) = f32x4{kacc[0], kacc[1], kacc[2], kacc[3]}; *lp<f32x4>(pa + 1024) = f32x4{kacc[4], kacc[5], kacc[6], kacc[7]}; }
      else { *lp<f32x4>(pa) = f32x4{kacc[8], kacc[9], kacc[10], kacc[11]}; *lp<f32x4>(pa + 1024) = f32x4{kacc[12], kacc[13], kacc[14], kacc[15]}; }
    }
    ST(4);
    BAR();
    q0 = *lp<u32x4>(L_QF + (w & 3) * 2048 + lane * 16);
    q1 = *lp<u32x4>(L_QF + (w & 3) * 2048 + 1024 + lane * 16);
    const unsigned yb = w * 16384, up = yb + 8448;
    {
      lds_t pa = lb(L_UT + (w ^ 1) * 2048 + lane * 16);
      f32x4 v0 = *lp<f32x4>(pa), v1 = *lp<f32x4>(pa + 1024);
      unsigned kf = L_KF + cl * 4160 + 64;
      if (!mh) {
        *lp<u32x2>(kf + r * 16 + h * 8) = u32x2{pk(v0[0] + kacc[0], v0[1] + kacc[1]), pk(v0[2] + kacc[2], v0[3] + kacc[3])};
        *lp<u32x2>(kf + 512 + r * 16 + h * 8) = u32x2{pk(v1[0] + kacc[4], v1[1] + kacc[5]), pk(v1[2] + kacc[6], v1[3] + kacc[7])};
      } else {
        float a0 = v0[0] + kacc[8], a4 = v0[1] + kacc[9], a8 = v0[2] + kacc[10];
        *lp<u32x4>(kf + 1024 + h * 512 + r * 16) = u32x4{pk(a0, a0), pk(a0, a4), pk(a4, a4), pk(a8, a8)};
        float c0 = v1[0] + kacc[12], c8 = v1[1] + kacc[13], d0 = v1[2] + kacc[14], d8 = v1[3] + kacc[15];
        bool odd = r & 1;
        *lp<u32x4>(kf + 2048 + h * 1024 + r * 16) = u32x4{pk(c0, c0), pk(c0, c0), pk(odd ? c8 : c0, c8), pk(c8, c8)};
        *lp<u32x4>(kf + 2560 + h * 1024 + r * 16) = u32x4{pk(d0, d0), pk(d0, d0), pk(odd ? d8 : d0, d8), pk(d8, d8)};
      }
      if (lane < 32) *lp<u32x4>(up + lane * 16) = u32x4{0};
    }
    BAR();
    ST(5);
    poll(ws + WS_F1 + (hq * 32 + r) * 128, tag);
    {
      unsigned vo = WS_UT + (hq * 256 + lane) * 1024 + (2 * qb + cl) * 16;
      u32x4 v[4];
      lds_t ul = lb(up + lane * 16);
#pragma unroll
      for (int i = 0; i < 4; ++i) v[i] = __builtin_amdgcn_raw_buffer_load_b128(wsr, vo, i * 65536, SC1);
#pragma unroll
      for (int i = 0; i < 4; ++i) *lp<u32x4>(ul + 512 + i * 1024) = v[i];
    }
    ST(6);
    {
      const unsigned fa0 = L_KF + cl * 4160 + 128 + 32 * g1 + 8 * pp - 64 * h - 32 - 8 * q4;
      const int qq = (r >> 2) - 1 + h;
      const unsigned ga0 = up + 128 * (4 + (qq >> 1)) + 32 * (r & 3) + 16 * (qq & 1);
      f32x16 ya[4];
#pragma unroll
      for (int k = 0; k < 4; ++k) ya[k] = f32x16{0};
      if (mh) conv_wave<1>(fa0, ga0, ya); else conv_wave<0>(fa0, ga0, ya);
      const unsigned tiles = mh ? 0x01020506u : 0x00030407u;
      f32x4 y[4];
      lds_t yw = lb(yb + lane * 4), yr = lb(yb + h * 4160 + r * 32), yo = lb(yb + (lane >> 2) * 4160 + 1024 + pp * 4);
#pragma unroll
      for (int i = 0; i < 2; ++i) {
        if (lane < 16) *lp<u32x4>(yb + (lane >> 1) * 1040 + (lane & 1) * 1024) = u32x4{0};
        CFENCE();
#pragma unroll
        for (int k = 0; k < 2; ++k)
#pragma unroll
          for (int j = 0; j < 16; ++j) *lp<float>(yw + k * 4160 + (j & 3) * 1044 + (j >> 2) * 256) = ya[2 * i + k][j];
        CFENCE();
        y[2 * i] = (*lp<f32x4>(yr) + *lp<f32x4>(yr + 1040)) + (*lp<f32x4>(yr + 2080) + *lp<f32x4>(yr + 3120));
        y[2 * i + 1] = (*lp<f32x4>(yr + 16) + *lp<f32x4>(yr + 1056)) + (*lp<f32x4>(yr + 2096) + *lp<f32x4>(yr + 3136));
        if (lane < 8)
          *lp<float>(L_OV + cl * 144 + (((tiles >> (16 * i + 8 * (lane >> 2))) & 7) + 1) * 16 + pp * 4) =
              *lp<float>(yo + 1040) + *lp<float>(yo + 2080) + *lp<float>(yo + 3120);
        CFENCE();
      }
      asm volatile("s_waitcnt lgkmcnt(0)" ::: "memory");
      if (lane == 0) *(volatile AS3 unsigned*)(size_t)(L_FL + w * 4) = 1;
#pragma unroll 1
      for (unsigned s = 0; s < SPIN_CAP; ++s) {
        if (__builtin_amdgcn_readfirstlane(*(volatile AS3 unsigned*)(size_t)(L_FL + (w ^ 1) * 4))) break;
        __builtin_amdgcn_s_sleep(1);
      }
      CFENCE();
#pragma unroll
      for (int i = 0; i < 2; ++i) {
        int tl = (tiles >> (16 * i + 8 * h)) & 7;
        f32x4 y0 = y[2 * i] + *lp<f32x4>(L_OV + cl * 144 + (r ? 0 : tl) * 16), y1 = y[2 * i + 1];
        __builtin_amdgcn_raw_buffer_store_b128(u32x4{pk(y0[0], y0[1]), pk(y0[2], y0[3]), pk(y1[0], y1[1]), pk(y1[2], y1[3])}, wsr,
                                               WS_YT + ch * 4096 + tl * 512 + r * 16, 0, SC1);
      }
    }
    asm volatile("s_waitcnt lgkmcnt(0)" ::: "memory");
    __builtin_amdgcn_s_setprio(2);
  }
  ST(7);

  const int head = 4 * hq + (w & 3);
  const int nst = w < 4 ? NA_STEPS : 64 - NA_STEPS;
  const unsigned kvo = WS_KV + head * KV_HEAD + (w < 4 ? 64 - NA_STEPS : 0) * 2048;
  const unsigned koff = lane * 16, voff = (lane & 15) * 16 + h * 256;
  float r0[9], r1[9], m0, m1, tm0 = 0.f, tm1 = 0.f;
  for (int mode = 0;; ++mode) {
    const u32x4 ones = {0x3F803F80u, 0x3F803F80u, 0x3F803F80u, 0x3F803F80u};
    u32x4 k0, k1, k2, k3 = ones, va0 = ones, va1 = ones, vb0 = ones, vb1 = ones, vc0 = ones, vc1 = ones, vd0 = ones, vd1 = ones;
    k0 = __builtin_amdgcn_raw_buffer_load_b128(wsr, koff, kvo, SC1);
    k1 = __builtin_amdgcn_raw_buffer_load_b128(wsr, koff + 2048, kvo, SC1);
    k2 = __builtin_amdgcn_raw_buffer_load_b128(wsr, koff, kvo + 4096, SC1);
    if (!g1) {
      va0 = __builtin_amdgcn_raw_buffer_load_b128(wsr, voff + 1024, kvo, SC1);
      va1 = __builtin_amdgcn_raw_buffer_load_b128(wsr, voff + 1536, kvo, SC1);
      vb0 = __builtin_amdgcn_raw_buffer_load_b128(wsr, voff + 3072, kvo, SC1);
      vb1 = __builtin_amdgcn_raw_buffer_load_b128(wsr, voff + 3584, kvo, SC1);
      vc0 = __builtin_amdgcn_raw_buffer_load_b128(wsr, voff + 1024, kvo + 4096, SC1);
      vc1 = __builtin_amdgcn_raw_buffer_load_b128(wsr, voff + 1536, kvo + 4096, SC1);
    }
    f32x16 z = {0};
    f32x16 s0 = mfma(k0, q0, z), s1 = mfma(k0, q1, z);
    float m = s0[0];
#pragma unroll
    for (int i = 0; i < 16; ++i) m = fmaxf(m, fmaxf(s0[i], s1[i]));
    {
      auto rr = __builtin_amdgcn_permlane32_swap(__float_as_uint(m), __float_as_uint(m), false, false);
      m = fmaxf(__uint_as_float(rr[0]), __uint_as_float(rr[1]));
    }
    if (mode) m = mode == 1 ? tm0 : tm1;
    f32x16 nm, o0 = z, o1 = z;
#pragma unroll
    for (int i = 0; i < 16; ++i) { nm[i] = -m; s0[i] -= m; }
    s1 = z;
    if (mode == 0 && w < 4) {
      VMW(0);
      unsigned old = 0;
      if (lane == 0) old = __hip_atomic_fetch_add(lp<unsigned>(L_FL + 16), 1u, __ATOMIC_RELAXED, __HIP_MEMORY_SCOPE_WORKGROUP);
      if (lane == 0 && old == 3) __hip_atomic_store((AS1 u64*)(ws + WS_F2 + (hq * 32 + qb) * 128), tag, __ATOMIC_RELAXED, __HIP_MEMORY_SCOPE_AGENT);
    }
    const char* kp = ws + kvo + 5 * 2048;
    const char* wp = wo + (hq >> 1) * 256;
    unsigned na = mode ? 0 : 4, nb = nst / 4 - na;
    const unsigned woff = (64 * w + (lane >> 4)) * 1024 + (((lane & 15) ^ ((lane >> 4) << 2)) << 4);
    asm volatile("s_waitcnt vmcnt(0)\n\ts_nop 4\n\t" ATT_LOOP_ASM
                 : "+{v[124:139]}"(o0), "+{v[140:155]}"(o1), "+{v[156:171]}"(s0), "+{v[172:187]}"(s1),
                   "+{v[188:191]}"(k0), "+{v[192:195]}"(va0), "+{v[196:199]}"(va1), "+{v[200:203]}"(k1), "+{v[204:207]}"(vb0),
                   "+{v[208:211]}"(vb1), "+{v[212:215]}"(k2), "+{v[216:219]}"(vc0), "+{v[220:223]}"(vc1), "+{v[224:227]}"(k3),
                   "+{v[228:231]}"(vd0), "+{v[232:235]}"(vd1), "+{s[40:41]}"(kp), "+{s44}"(na), "+{s45}"(nb), "+{s[46:47]}"(wp)
                 : "{v[100:103]}"(q0), "{v[104:107]}"(q1), "{v[108:123]}"(nm), "{v236}"(koff), "{v237}"(voff), "{v238}"(0xDDDD),
                   "{v239}"(woff), "{s[42:43]}"(0x0000FFFF0000FFFFull), "{s48}"(w * 16384)
                 : "scc", "memory");
    if (mode != 2) {
#pragma unroll
      for (int i = 0; i < 9; ++i) r0[i] = o0[i];
      m0 = m;
    }
    if (mode != 1) {
#pragma unroll
      for (int i = 0; i < 9; ++i) r1[i] = o1[i];
      m1 = m;
    }
    if (mode == 2) break;
    if (mode == 0) {
      float big = 0.f;
#pragma unroll
      for (int i = 0; i < 8; ++i) big += fabsf(o0[i]) + fabsf(o1[i]);
      bool ok = big < 1e37f && o0[8] > 5e-20f && o0[8] < 1e30f && o1[8] > 5e-20f && o1[8] < 1e30f;
      if (__all(ok)) break;
      tm0 = tm1 = -__builtin_inff();
#pragma unroll 1
      for (int j = 0; j < nst; ++j) {
        u32x4 kk = __builtin_amdgcn_raw_buffer_load_b128(wsr, koff, kvo + j * 2048, SC1);
        f32x16 a = mfma(kk, q0, z), c = mfma(kk, q1, z);
#pragma unroll
        for (int i = 0; i < 16; ++i) { tm0 = fmaxf(tm0, a[i]); tm1 = fmaxf(tm1, c[i]); }
      }
      tm0 = fmaxf(tm0, __shfl_xor(tm0, 32));
      tm1 = fmaxf(tm1, __shfl_xor(tm1, 32));
    }
  }
  __builtin_amdgcn_s_setprio(0);
  ST(8);

  const int tt = 2 * qb + (hq & 1);
  if (w >= 4) {
#pragma unroll
    for (int i = 0; i < 9; ++i) { float f = r0[i]; r0[i] = r1[i]; r1[i] = f; }
    float f = m0; m0 = m1; m1 = f;
  }
  {
    unsigned ea = (w < 4 ? L_EX : L_UT) + (w & 3) * 2560;
    *lp<f32x4>(ea + lane * 16) = f32x4{r1[0], r1[1], r1[2], r1[3]};
    *lp<f32x4>(ea + 1024 + lane * 16) = f32x4{r1[4], r1[5], r1[6], r1[7]};
    *lp<f32x2>(ea + 2048 + lane * 8) = f32x2{r1[8], m1};
  }
  BAR();
  ST(9);
  {
    unsigned ea = (w < 4 ? L_UT : L_EX) + (w & 3) * 2560;
    f32x4 a0 = *lp<f32x4>(ea + lane * 16), a1 = *lp<f32x4>(ea + 1024 + lane * 16);
    f32x2 lm = *lp<f32x2>(ea + 2048 + lane * 8);
    float M = fmaxf(m0, lm[1]);
    float fa = __builtin_amdgcn_exp2f(m0 - M), fb = __builtin_amdgcn_exp2f(lm[1] - M);
    float inv = __builtin_amdgcn_rcpf(fa * r0[8] + fb * lm[0]);
    fa *= inv; fb *= inv;
    u32x4 o = {pk(fa * r0[0] + fb * a0[0], fa * r0[1] + fb * a0[1]), pk(fa * r0[2] + fb * a0[2], fa * r0[3] + fb * a0[3]),
               pk(fa * r0[4] + fb * a1[0], fa * r0[5] + fb * a1[1]), pk(fa * r0[6] + fb * a1[2], fa * r0[7] + fb * a1[3])};
#pragma unroll
    for (int i = 0; i < 4; ++i) o[i] = min(o[i], 0xFFFFFFFEu);
    __builtin_amdgcn_raw_buffer_store_b128(o, wsr, WS_O + ((2 * qb + (w >> 2)) * 32 + head) * 1024 + lane * 16, 0, SC1);
  }
  u32x4 us = *(const u32x4*)(u + (qb * 64 + lane) * 1024 + hq * 128 + w * 16);
  u32x4 ds = *(const u32x4*)(Dsk + hq * 128 + w * 16);
  u32x4 wf[16];
  {
    const lds_t a = lb((w >> 2) * 65536 + (4 * h + q4) * 256 + ((64 * (w & 3) + 32 * g1 + 8 * (((pp & 1) << 1) | (pp >> 1))) ^ (64 * q4)));
#pragma unroll
    for (int i = 0; i < 16; ++i) wf[i] = trd2(a + i * 4096, a + i * 4096 + 2048);
  }
  poll(ws + (lane < 4 ? WS_F2 + (hq * 32 + 4 * w + lane) * 128 : WS_F1 + (w * 32 + qb) * 128), tag);
  u32x4 yv = __builtin_amdgcn_raw_buffer_load_b128(wsr, WS_YT + (hq * 64 + 8 * w + (lane >> 3)) * 4096 + qb * 128 + (lane & 7) * 16, 0, SC1);
  BAR();
  ST(10);
  {
    unsigned yl = w * 1024;
    *lp<u32x4>(yl + lane * 16) = yv;
    lds_t a = lb(yl + q4 * 128 + (lane >> 4) * 32 + pp * 8);
    u32x4 y = trd2(a, a + 512), o;
#pragma unroll
    for (int i = 0; i < 4; ++i) {
      unsigned sk = pk(b2f(us[i] & 0xFFFF) * b2f(ds[i] & 0xFFFF), b2f(us[i] >> 16) * b2f(ds[i] >> 16));
      o[i] = pk(b2f(y[i] & 0xFFFF) + b2f(sk & 0xFFFF), b2f(y[i] >> 16) + b2f(sk >> 16));
    }
    *(u32x4*)(out + (qb * 64 + lane) * 2048 + hq * 128 + w * 16) = o;
  }
  ST(11);
  {
    u32x4 v[4];
    const unsigned vo = WS_O + (tt * 32 + 4 * w) * 1024 + lane * 16;
    const lds_t ol = lb(L_S + 4 * w * 1024 + lane * 16);
  #pragma unroll 1
  for (unsigned s = 0; s < SPIN_CAP; ++s) {
      CFENCE();
      unsigned mx = 0;
#pragma unroll
      for (int i = 0; i < 4; ++i) v[i] = __builtin_amdgcn_raw_buffer_load_b128(wsr, vo + i * 1024, 0, SC1);
#pragma unroll
      for (int i = 0; i < 4; ++i) mx = max(max(mx, max(v[i][0], v[i][1])), max(v[i][2], v[i][3]));
      if (__all(mx != 0xFFFFFFFFu)) break;
    }
#pragma unroll
    for (int i = 0; i < 4; ++i) *lp<u32x4>(ol + i * 1024) = v[i];
  }
  BAR();
  ST(12);
  if (b == 0 && threadIdx.x == 0) __hip_atomic_store((AS1 unsigned*)ws, epoch, __ATOMIC_RELAXED, __HIP_MEMORY_SCOPE_AGENT);
  {
    f32x16 acc = {0};
    const lds_t ob = lb(L_S + (w >> 2) * 16384 + lane * 16);
#pragma unroll
    for (int i = 0; i < 16; ++i) acc = mfma(wf[i], *lp<u32x4>(ob + i * 1024), acc);
    lds_t ea = lb(16384 + w * 2048 + lane * 16);
    f32x4 k0, k1;
    if (w < 4) {
      *lp<f32x4>(ea) = f32x4{acc[8], acc[9], acc[10], acc[11]}; *lp<f32x4>(ea + 1024) = f32x4{acc[12], acc[13], acc[14], acc[15]};
      k0 = f32x4{acc[0], acc[1], acc[2], acc[3]}; k1 = f32x4{acc[4], acc[5], acc[6], acc[7]};
    } else {
      *lp<f32x4>(ea) = f32x4{acc[0], acc[1], acc[2], acc[3]}; *lp<f32x4>(ea + 1024) = f32x4{acc[4], acc[5], acc[6], acc[7]};
      k0 = f32x4{acc[8], acc[9], acc[10], acc[11]}; k1 = f32x4{acc[12], acc[13], acc[14], acc[15]};
    }
    BAR();
    ea = lb(16384 + (w ^ 4) * 2048 + lane * 16);
    k0 += *lp<f32x4>(ea); k1 += *lp<f32x4>(ea + 1024);
    *(u32x4*)(out + (tt * 32 + r) * 2048 + 1024 + (hq >> 1) * 256 + (w & 3) * 64 + (w >> 2) * 32 + h * 16) =
        u32x4{pk(k0[0], k0[1]), pk(k0[2], k0[3]), pk(k1[0], k1[1]), pk(k1[2], k1[3])};
  }
  ST(13);
}

extern "C" void kernel_launch(void* const* d_in, const int* in_sizes, int n_in, void* d_out, int out_size, void* d_ws,
                              size_t ws_size, hipStream_t stream) {
  fused<<<256, 512, 0, stream>>>((const char*)d_in[0], (const u16*)d_in[1], (const u16*)d_in[2], (const u16*)d_in[3],
                                 (const u16*)d_in[4], (const u16*)d_in[5], (const u16*)d_in[6], (const u16*)d_in[7],
                                 (const char*)d_in[8], (const char*)d_in[9], (const char*)d_in[10], (const char*)d_in[11],
                                 (char*)d_out, (char*)d_ws);
}
```

```cpp
#include <hip/hip_runtime.h>
typedef float f32x16 __attribute__((ext_vector_type(16)));
typedef float f32x4 __attribute__((ext_vector_type(4)));
typedef float f32x2 __attribute__((ext_vector_type(2)));
typedef __bf16 bf16x8 __attribute__((ext_vector_type(8)));
typedef __bf16 bf16x2 __attribute__((ext_vector_type(2)));
typedef short s16x4 __attribute__((ext_vector_type(4)));
typedef unsigned u32x4 __attribute__((ext_vector_type(4)));
typedef unsigned u32x2 __attribute__((ext_vector_type(2)));
typedef unsigned short u16;
typedef unsigned long long u64;
#define AS3 __attribute__((address_space(3)))
#define AS1 __attribute__((address_space(1)))
#define DEV __device__ __forceinline__

#define ATT_LOOP_ASM \
  "s_add_u32 s50, s40, 8192\n\t" \
  "s_addc_u32 s51, s41, 0\n\t" \
  "global_load_dwordx4 v[144:147], v240, s[40:41] offset:-4096 sc1\n\t" \
  "s_mov_b64 exec, s[42:43]\n\t" \
  "global_load_dwordx4 v[148:151], v241, s[40:41] offset:-3072 sc1\n\t" \
  "global_load_dwordx4 v[152:155], v241, s[40:41] offset:-2560 sc1\n\t" \
  "s_mov_b64 exec, -1\n\t" \
  "global_load_dwordx4 v[156:159], v240, s[40:41] offset:-2048 sc1\n\t" \
  "s_mov_b64 exec, s[42:43]\n\t" \
  "global_load_dwordx4 v[160:163], v241, s[40:41] offset:-1024 sc1\n\t" \
  "global_load_dwordx4 v[164:167], v241, s[40:41] offset:-512 sc1\n\t" \
  "s_mov_b64 exec, -1\n\t" \
  "global_load_dwordx4 v[168:171], v240, s[40:41] offset:0 sc1\n\t" \
  "s_mov_b64 exec, s[42:43]\n\t" \
  "global_load_dwordx4 v[172:175], v241, s[40:41] offset:1024 sc1\n\t" \
  "global_load_dwordx4 v[176:179], v241, s[40:41] offset:1536 sc1\n\t" \
  "s_mov_b64 exec, -1\n\t" \
  "global_load_dwordx4 v[180:183], v240, s[40:41] offset:2048 sc1\n\t" \
  "s_mov_b64 exec, s[42:43]\n\t" \
  "global_load_dwordx4 v[184:187], v241, s[40:41] offset:3072 sc1\n\t" \
  "global_load_dwordx4 v[188:191], v241, s[40:41] offset:3584 sc1\n\t" \
  "s_mov_b64 exec, -1\n\t" \
  "global_load_dwordx4 v[192:195], v240, s[50:51] offset:-4096 sc1\n\t" \
  "s_mov_b64 exec, s[42:43]\n\t" \
  "global_load_dwordx4 v[196:199], v241, s[50:51] offset:-3072 sc1\n\t" \
  "global_load_dwordx4 v[200:203], v241, s[50:51] offset:-2560 sc1\n\t" \
  "s_mov_b64 exec, -1\n\t" \
  "global_load_dwordx4 v[204:207], v240, s[50:51] offset:-2048 sc1\n\t" \
  "s_mov_b64 exec, s[42:43]\n\t" \
  "global_load_dwordx4 v[208:211], v241, s[50:51] offset:-1024 sc1\n\t" \
  "global_load_dwordx4 v[212:215], v241, s[50:51] offset:-512 sc1\n\t" \
  "s_mov_b64 exec, -1\n\t" \
  "global_load_dwordx4 v[216:219], v240, s[50:51] offset:0 sc1\n\t" \
  "s_mov_b64 exec, s[42:43]\n\t" \
  "global_load_dwordx4 v[220:223], v241, s[50:51] offset:1024 sc1\n\t" \
  "global_load_dwordx4 v[224:227], v241, s[50:51] offset:1536 sc1\n\t" \
  "s_mov_b64 exec, -1\n\t" \
  "global_load_dwordx4 v[228:231], v240, s[50:51] offset:2048 sc1\n\t" \
  "s_mov_b64 exec, s[42:43]\n\t" \
  "global_load_dwordx4 v[232:235], v241, s[50:51] offset:3072 sc1\n\t" \
  "global_load_dwordx4 v[236:239], v241, s[50:51] offset:3584 sc1\n\t" \
  "s_mov_b64 exec, -1\n\t" \
  "s_add_u32 s40, s40, 16384\n\t" \
  "s_addc_u32 s41, s41, 0\n\t" \
  "s_add_u32 s50, s50, 16384\n\t" \
  "s_addc_u32 s51, s51, 0\n\t" \
  "s_not_b64 exec, s[42:43]\n\t" \
  "v_mov_b64 v[148:149], s[52:53]\n\t" \
  "v_mov_b64 v[150:151], s[52:53]\n\t" \
  "v_mov_b64 v[152:153], s[52:53]\n\t" \
  "v_mov_b64 v[154:155], s[52:53]\n\t" \
  "v_mov_b64 v[160:161], s[52:53]\n\t" \
  "v_mov_b64 v[162:163], s[52:53]\n\t" \
  "v_mov_b64 v[164:165], s[52:53]\n\t" \
  "v_mov_b64 v[166:167], s[52:53]\n\t" \
  "v_mov_b64 v[172:173], s[52:53]\n\t" \
  "v_mov_b64 v[174:175], s[52:53]\n\t" \
  "v_mov_b64 v[176:177], s[52:53]\n\t" \
  "v_mov_b64 v[178:179], s[52:53]\n\t" \
  "v_mov_b64 v[184:185], s[52:53]\n\t" \
  "v_mov_b64 v[186:187], s[52:53]\n\t" \
  "v_mov_b64 v[188:189], s[52:53]\n\t" \
  "v_mov_b64 v[190:191], s[52:53]\n\t" \
  "v_mov_b64 v[196:197], s[52:53]\n\t" \
  "v_mov_b64 v[198:199], s[52:53]\n\t" \
  "v_mov_b64 v[200:201], s[52:53]\n\t" \
  "v_mov_b64 v[202:203], s[52:53]\n\t" \
  "v_mov_b64 v[208:209], s[52:53]\n\t" \
  "v_mov_b64 v[210:211], s[52:53]\n\t" \
  "v_mov_b64 v[212:213], s[52:53]\n\t" \
  "v_mov_b64 v[214:215], s[52:53]\n\t" \
  "v_mov_b64 v[220:221], s[52:53]\n\t" \
  "v_mov_b64 v[222:223], s[52:53]\n\t" \
  "v_mov_b64 v[224:225], s[52:53]\n\t" \
  "v_mov_b64 v[226:227], s[52:53]\n\t" \
  "v_mov_b64 v[232:233], s[52:53]\n\t" \
  "v_mov_b64 v[234:235], s[52:53]\n\t" \
  "v_mov_b64 v[236:237], s[52:53]\n\t" \
  "v_mov_b64 v[238:239], s[52:53]\n\t" \
  "s_mov_b64 exec, -1\n\t" \
  "v_mov_b64 v[80:81], 0\n\t" \
  "v_mov_b64 v[82:83], 0\n\t" \
  "v_mov_b64 v[84:85], 0\n\t" \
  "v_mov_b64 v[86:87], 0\n\t" \
  "v_mov_b64 v[88:89], 0\n\t" \
  "v_mov_b64 v[96:97], 0\n\t" \
  "v_mov_b64 v[98:99], 0\n\t" \
  "v_mov_b64 v[100:101], 0\n\t" \
  "v_mov_b64 v[102:103], 0\n\t" \
  "v_mov_b64 v[104:105], 0\n\t" \
  "s_mov_b32 m0, s48\n\t" \
  "s_mov_b32 s44, 4\n\t" \
  "s_cmp_lg_u32 s45, 0\n\t" \
  "s_waitcnt vmcnt(23)\n\t" \
  "s_cbranch_scc1 3f\n\t" \
  "v_mfma_f32_32x32x16_bf16 v[112:127], v[144:147], v[56:59], 0\n\t" \
  "v_mfma_f32_32x32x16_bf16 v[128:143], v[144:147], v[60:63], 0\n\t" \
  "s_nop 7\n\t" \
  "s_nop 3\n\t" \
  "v_max3_f32 v243, v112, v113, v114\n\t" \
  "v_max3_f32 v243, v243, v115, v116\n\t" \
  "v_max3_f32 v243, v243, v117, v118\n\t" \
  "v_max3_f32 v243, v243, v119, v120\n\t" \
  "v_max3_f32 v243, v243, v121, v122\n\t" \
  "v_max3_f32 v243, v243, v123, v124\n\t" \
  "v_max3_f32 v243, v243, v125, v126\n\t" \
  "s_nop 1\n\t" \
  "v_max3_f32 v243, v243, v127, v128\n\t" \
  "v_max3_f32 v243, v243, v129, v130\n\t" \
  "v_max3_f32 v243, v243, v131, v132\n\t" \
  "v_max3_f32 v243, v243, v133, v134\n\t" \
  "v_max3_f32 v243, v243, v135, v136\n\t" \
  "v_max3_f32 v243, v243, v137, v138\n\t" \
  "v_max3_f32 v243, v243, v139, v140\n\t" \
  "v_max3_f32 v243, v243, v141, v142\n\t" \
  "v_max_f32 v243, v243, v143\n\t" \
  "v_mov_b32 v64, v243\n\t" \
  "s_nop 1\n\t" \
  "v_permlane32_swap_b32 v243, v64\n\t" \
  "s_nop 1\n\t" \
  "v_max_f32 v243, v243, v64\n\t" \
  "3:\n\t" \
  "v_xor_b32 v64, 0x80000000, v243\n\t" \
  "v_mov_b32 v65, v64\n\t" \
  "v_mov_b64 v[66:67], v[64:65]\n\t" \
  "v_mov_b64 v[68:69], v[64:65]\n\t" \
  "v_mov_b64 v[70:71], v[64:65]\n\t" \
  "v_mov_b64 v[72:73], v[64:65]\n\t" \
  "v_mov_b64 v[74:75], v[64:65]\n\t" \
  "v_mov_b64 v[76:77], v[64:65]\n\t" \
  "v_mov_b64 v[78:79], v[64:65]\n\t" \
  "s_nop 1\n\t" \
  "v_mfma_f32_32x32x16_bf16 v[112:127], v[144:147], v[56:59], v[64:79]\n\t" \
  "s_waitcnt vmcnt(21)\n\t" \
  "s_nop 7\n\t" \
  "s_nop 3\n\t" \
  "v_exp_f32 v112, v112\n\t" \
  "v_exp_f32 v113, v113\n\t" \
  "s_cmp_eq_u32 s44, 1\n\t" \
  "1:\n\t" \
  ATT_ST \
  "v_exp_f32 v114, v114\n\t" \
  "v_exp_f32 v115, v115\n\t" \
  "v_exp_f32 v116, v116\n\t" \
  "v_mfma_f32_32x32x16_bf16 v[128:143], v[144:147], v[60:63], v[64:79]\n\t" \
  "v_exp_f32 v117, v117\n\t" \
  "v_exp_f32 v118, v118\n\t" \
  "global_load_lds_dwordx4 v244, s[46:47] offset:0\n\t" \
  "v_exp_f32 v119, v119\n\t" \
  "global_load_lds_dwordx4 v245, s[46:47] offset:1024\n\t" \
  "v_exp_f32 v120, v120\n\t" \
  "global_load_lds_dwordx4 v246, s[46:47] offset:2048\n\t" \
  "v_exp_f32 v121, v121\n\t" \
  "global_load_lds_dwordx4 v247, s[46:47] offset:3072\n\t" \
  "v_exp_f32 v122, v122\n\t" \
  "v_smfmac_f32_32x32x32_bf16 v[80:95], v[148:151], v[112:119], v242\n\t" \
  "v_exp_f32 v123, v123\n\t" \
  "v_exp_f32 v124, v124\n\t" \
  "v_exp_f32 v125, v125\n\t" \
  "v_exp_f32 v126, v126\n\t" \
  "v_exp_f32 v127, v127\n\t" \
  "v_exp_f32 v128, v128\n\t" \
  "v_exp_f32 v129, v129\n\t" \
  "v_smfmac_f32_32x32x32_bf16 v[80:95], v[152:155], v[120:127], v242\n\t" \
  "v_exp_f32 v130, v130\n\t" \
  "v_exp_f32 v131, v131\n\t" \
  "v_exp_f32 v132, v132\n\t" \
  "s_waitcnt vmcnt(22)\n\t" \
  "v_mfma_f32_32x32x16_bf16 v[112:127], v[156:159], v[56:59], v[64:79]\n\t" \
  "v_exp_f32 v133, v133\n\t" \
  "v_exp_f32 v134, v134\n\t" \
  "v_exp_f32 v135, v135\n\t" \
  "v_exp_f32 v136, v136\n\t" \
  "v_exp_f32 v137, v137\n\t" \
  "v_exp_f32 v138, v138\n\t" \
  "v_smfmac_f32_32x32x32_bf16 v[96:111], v[148:151], v[128:135], v242\n\t" \
  "v_exp_f32 v139, v139\n\t" \
  "v_exp_f32 v140, v140\n\t" \
  "v_exp_f32 v141, v141\n\t" \
  "v_exp_f32 v142, v142\n\t" \
  "v_exp_f32 v143, v143\n\t" \
  "v_exp_f32 v112, v112\n\t" \
  "v_exp_f32 v113, v113\n\t" \
  "v_smfmac_f32_32x32x32_bf16 v[96:111], v[152:155], v[136:143], v242\n\t" \
  "s_cbranch_scc1 20f\n\t" \
  "global_load_dwordx4 v[144:147], v240, s[40:41] offset:-4096 sc1\n\t" \
  "s_mov_b64 exec, s[42:43]\n\t" \
  "global_load_dwordx4 v[148:151], v241, s[40:41] offset:-3072 sc1\n\t" \
  "global_load_dwordx4 v[152:155], v241, s[40:41] offset:-2560 sc1\n\t" \
  "s_mov_b64 exec, -1\n\t" \
  "10:\n\t" \
  "v_exp_f32 v114, v114\n\t" \
  "v_exp_f32 v115, v115\n\t" \
  "v_exp_f32 v116, v116\n\t" \
  "v_mfma_f32_32x32x16_bf16 v[128:143], v[156:159], v[60:63], v[64:79]\n\t" \
  "v_exp_f32 v117, v117\n\t" \
  "v_exp_f32 v118, v118\n\t" \
  "v_exp_f32 v119, v119\n\t" \
  "v_exp_f32 v120, v120\n\t" \
  "v_exp_f32 v121, v121\n\t" \
  "v_exp_f32 v122, v122\n\t" \
  "v_smfmac_f32_32x32x32_bf16 v[80:95], v[160:163], v[112:119], v242\n\t" \
  "v_exp_f32 v123, v123\n\t" \
  "v_exp_f32 v124, v124\n\t" \
  "v_exp_f32 v125, v125\n\t" \
  "v_exp_f32 v126, v126\n\t" \
  "v_exp_f32 v127, v127\n\t" \
  "v_exp_f32 v128, v128\n\t" \
  "v_exp_f32 v129, v129\n\t" \
  "v_smfmac_f32_32x32x32_bf16 v[80:95], v[164:167], v[120:127], v242\n\t" \
  "v_exp_f32 v130, v130\n\t" \
  "v_exp_f32 v131, v131\n\t" \
  "v_exp_f32 v132, v132\n\t" \
  "s_waitcnt vmcnt(22)\n\t" \
  "v_mfma_f32_32x32x16_bf16 v[112:127], v[168:171], v[56:59], v[64:79]\n\t" \
  "v_exp_f32 v133, v133\n\t" \
  "v_exp_f32 v134, v134\n\t" \
  "v_exp_f32 v135, v135\n\t" \
  "v_exp_f32 v136, v136\n\t" \
  "v_exp_f32 v137, v137\n\t" \
  "v_exp_f32 v138, v138\n\t" \
  "v_smfmac_f32_32x32x32_bf16 v[96:111], v[160:163], v[128:135], v242\n\t" \
  "v_exp_f32 v139, v139\n\t" \
  "v_exp_f32 v140, v140\n\t" \
  "v_exp_f32 v141, v141\n\t" \
  "v_exp_f32 v142, v142\n\t" \
  "v_exp_f32 v143, v143\n\t" \
  "v_exp_f32 v112, v112\n\t" \
  "v_exp_f32 v113, v113\n\t" \
  "v_smfmac_f32_32x32x32_bf16 v[96:111], v[164:167], v[136:143], v242\n\t" \
  "s_cbranch_scc1 21f\n\t" \
  "global_load_dwordx4 v[156:159], v240, s[40:41] offset:-2048 sc1\n\t" \
  "s_mov_b64 exec, s[42:43]\n\t" \
  "global_load_dwordx4 v[160:163], v241, s[40:41] offset:-1024 sc1\n\t" \
  "global_load_dwordx4 v[164:167], v241, s[40:41] offset:-512 sc1\n\t" \
  "s_mov_b64 exec, -1\n\t" \
  "11:\n\t" \
  "v_exp_f32 v114, v114\n\t" \
  "v_exp_f32 v115, v115\n\t" \
  "v_exp_f32 v116, v116\n\t" \
  "v_mfma_f32_32x32x16_bf16 v[128:143], v[168:171], v[60:63], v[64:79]\n\t" \
  "v_exp_f32 v117, v117\n\t" \
  "v_exp_f32 v118, v118\n\t" \
  "v_exp_f32 v119, v119\n\t" \
  "v_exp_f32 v120, v120\n\t" \
  "v_exp_f32 v121, v121\n\t" \
  "v_exp_f32 v122, v122\n\t" \
  "v_smfmac_f32_32x32x32_bf16 v[80:95], v[172:175], v[112:119], v242\n\t" \
  "v_exp_f32 v123, v123\n\t" \
  "v_exp_f32 v124, v124\n\t" \
  "v_exp_f32 v125, v125\n\t" \
  "v_exp_f32 v126, v126\n\t" \
  "v_exp_f32 v127, v127\n\t" \
  "v_exp_f32 v128, v128\n\t" \
  "v_exp_f32 v129, v129\n\t" \
  "v_smfmac_f32_32x32x32_bf16 v[80:95], v[176:179], v[120:127], v242\n\t" \
  "v_exp_f32 v130, v130\n\t" \
  "v_exp_f32 v131, v131\n\t" \
  "v_exp_f32 v132, v132\n\t" \
  "s_waitcnt vmcnt(22)\n\t" \
  "v_mfma_f32_32x32x16_bf16 v[112:127], v[180:183], v[56:59], v[64:79]\n\t" \
  "v_exp_f32 v133, v133\n\t" \
  "v_exp_f32 v134, v134\n\t" \
  "v_exp_f32 v135, v135\n\t" \
  "v_exp_f32 v136, v136\n\t" \
  "v_exp_f32 v137, v137\n\t" \
  "v_exp_f32 v138, v138\n\t" \
  "v_smfmac_f32_32x32x32_bf16 v[96:111], v[172:175], v[128:135], v242\n\t" \
  "v_exp_f32 v139, v139\n\t" \
  "v_exp_f32 v140, v140\n\t" \
  "v_exp_f32 v141, v141\n\t" \
  "v_exp_f32 v142, v142\n\t" \
  "v_exp_f32 v143, v143\n\t" \
  "v_exp_f32 v112, v112\n\t" \
  "v_exp_f32 v113, v113\n\t" \
  "v_smfmac_f32_32x32x32_bf16 v[96:111], v[176:179], v[136:143], v242\n\t" \
  "s_cbranch_scc1 22f\n\t" \
  "global_load_dwordx4 v[168:171], v240, s[40:41] offset:0 sc1\n\t" \
  "s_mov_b64 exec, s[42:43]\n\t" \
  "global_load_dwordx4 v[172:175], v241, s[40:41] offset:1024 sc1\n\t" \
  "global_load_dwordx4 v[176:179], v241, s[40:41] offset:1536 sc1\n\t" \
  "s_mov_b64 exec, -1\n\t" \
  "12:\n\t" \
  "v_exp_f32 v114, v114\n\t" \
  "v_exp_f32 v115, v115\n\t" \
  "v_exp_f32 v116, v116\n\t" \
  "v_mfma_f32_32x32x16_bf16 v[128:143], v[180:183], v[60:63], v[64:79]\n\t" \
  "v_exp_f32 v117, v117\n\t" \
  "v_exp_f32 v118, v118\n\t" \
  "v_exp_f32 v119, v119\n\t" \
  "v_exp_f32 v120, v120\n\t" \
  "v_exp_f32 v121, v121\n\t" \
  "v_exp_f32 v122, v122\n\t" \
  "v_smfmac_f32_32x32x32_bf16 v[80:95], v[184:187], v[112:119], v242\n\t" \
  "v_exp_f32 v123, v123\n\t" \
  "v_exp_f32 v124, v124\n\t" \
  "v_exp_f32 v125, v125\n\t" \
  "v_exp_f32 v126, v126\n\t" \
  "v_exp_f32 v127, v127\n\t" \
  "v_exp_f32 v128, v128\n\t" \
  "v_exp_f32 v129, v129\n\t" \
  "v_smfmac_f32_32x32x32_bf16 v[80:95], v[188:191], v[120:127], v242\n\t" \
  "v_exp_f32 v130, v130\n\t" \
  "v_exp_f32 v131, v131\n\t" \
  "v_exp_f32 v132, v132\n\t" \
  "s_waitcnt vmcnt(22)\n\t" \
  "v_mfma_f32_32x32x16_bf16 v[112:127], v[192:195], v[56:59], v[64:79]\n\t" \
  "v_exp_f32 v133, v133\n\t" \
  "v_exp_f32 v134, v134\n\t" \
  "v_exp_f32 v135, v135\n\t" \
  "v_exp_f32 v136, v136\n\t" \
  "v_exp_f32 v137, v137\n\t" \
  "v_exp_f32 v138, v138\n\t" \
  "v_smfmac_f32_32x32x32_bf16 v[96:111], v[184:187], v[128:135], v242\n\t" \
  "v_exp_f32 v139, v139\n\t" \
  "v_exp_f32 v140, v140\n\t" \
  "v_exp_f32 v141, v141\n\t" \
  "v_exp_f32 v142, v142\n\t" \
  "v_exp_f32 v143, v143\n\t" \
  "v_exp_f32 v112, v112\n\t" \
  "v_exp_f32 v113, v113\n\t" \
  "v_smfmac_f32_32x32x32_bf16 v[96:111], v[188:191], v[136:143], v242\n\t" \
  "s_cbranch_scc1 23f\n\t" \
  "global_load_dwordx4 v[180:183], v240, s[40:41] offset:2048 sc1\n\t" \
  "s_mov_b64 exec, s[42:43]\n\t" \
  "global_load_dwordx4 v[184:187], v241, s[40:41] offset:3072 sc1\n\t" \
  "global_load_dwordx4 v[188:191], v241, s[40:41] offset:3584 sc1\n\t" \
  "s_mov_b64 exec, -1\n\t" \
  "13:\n\t" \
  "v_exp_f32 v114, v114\n\t" \
  "v_exp_f32 v115, v115\n\t" \
  "v_exp_f32 v116, v116\n\t" \
  "v_mfma_f32_32x32x16_bf16 v[128:143], v[192:195], v[60:63], v[64:79]\n\t" \
  "v_exp_f32 v117, v117\n\t" \
  "v_exp_f32 v118, v118\n\t" \
  "v_exp_f32 v119, v119\n\t" \
  "v_exp_f32 v120, v120\n\t" \
  "v_exp_f32 v121, v121\n\t" \
  "v_exp_f32 v122, v122\n\t" \
  "v_smfmac_f32_32x32x32_bf16 v[80:95], v[196:199], v[112:119], v242\n\t" \
  "v_exp_f32 v123, v123\n\t" \
  "v_exp_f32 v124, v124\n\t" \
  "v_exp_f32 v125, v125\n\t" \
  "v_exp_f32 v126, v126\n\t" \
  "v_exp_f32 v127, v127\n\t" \
  "v_exp_f32 v128, v128\n\t" \
  "v_exp_f32 v129, v129\n\t" \
  "v_smfmac_f32_32x32x32_bf16 v[80:95], v[200:203], v[120:127], v242\n\t" \
  "v_exp_f32 v130, v130\n\t" \
  "v_exp_f32 v131, v131\n\t" \
  "v_exp_f32 v132, v132\n\t" \
  "s_waitcnt vmcnt(22)\n\t" \
  "v_mfma_f32_32x32x16_bf16 v[112:127], v[204:207], v[56:59], v[64:79]\n\t" \
  "v_exp_f32 v133, v133\n\t" \
  "v_exp_f32 v134, v134\n\t" \
  "v_exp_f32 v135, v135\n\t" \
  "v_exp_f32 v136, v136\n\t" \
  "v_exp_f32 v137, v137\n\t" \
  "v_exp_f32 v138, v138\n\t" \
  "v_smfmac_f32_32x32x32_bf16 v[96:111], v[196:199], v[128:135], v242\n\t" \
  "v_exp_f32 v139, v139\n\t" \
  "v_exp_f32 v140, v140\n\t" \
  "v_exp_f32 v141, v141\n\t" \
  "v_exp_f32 v142, v142\n\t" \
  "v_exp_f32 v143, v143\n\t" \
  "v_exp_f32 v112, v112\n\t" \
  "v_exp_f32 v113, v113\n\t" \
  "v_smfmac_f32_32x32x32_bf16 v[96:111], v[200:203], v[136:143], v242\n\t" \
  "s_cbranch_scc1 24f\n\t" \
  "global_load_dwordx4 v[192:195], v240, s[50:51] offset:-4096 sc1\n\t" \
  "s_mov_b64 exec, s[42:43]\n\t" \
  "global_load_dwordx4 v[196:199], v241, s[50:51] offset:-3072 sc1\n\t" \
  "global_load_dwordx4 v[200:203], v241, s[50:51] offset:-2560 sc1\n\t" \
  "s_mov_b64 exec, -1\n\t" \
  "14:\n\t" \
  "v_exp_f32 v114, v114\n\t" \
  "v_exp_f32 v115, v115\n\t" \
  "v_exp_f32 v116, v116\n\t" \
  "v_mfma_f32_32x32x16_bf16 v[128:143], v[204:207], v[60:63], v[64:79]\n\t" \
  "v_exp_f32 v117, v117\n\t" \
  "v_exp_f32 v118, v118\n\t" \
  "v_exp_f32 v119, v119\n\t" \
  "v_exp_f32 v120, v120\n\t" \
  "v_exp_f32 v121, v121\n\t" \
  "v_exp_f32 v122, v122\n\t" \
  "v_smfmac_f32_32x32x32_bf16 v[80:95], v[208:211], v[112:119], v242\n\t" \
  "v_exp_f32 v123, v123\n\t" \
  "v_exp_f32 v124, v124\n\t" \
  "v_exp_f32 v125, v125\n\t" \
  "v_exp_f32 v126, v126\n\t" \
  "v_exp_f32 v127, v127\n\t" \
  "v_exp_f32 v128, v128\n\t" \
  "v_exp_f32 v129, v129\n\t" \
  "v_smfmac_f32_32x32x32_bf16 v[80:95], v[212:215], v[120:127], v242\n\t" \
  "v_exp_f32 v130, v130\n\t" \
  "v_exp_f32 v131, v131\n\t" \
  "v_exp_f32 v132, v132\n\t" \
  "s_waitcnt vmcnt(22)\n\t" \
  "v_mfma_f32_32x32x16_bf16 v[112:127], v[216:219], v[56:59], v[64:79]\n\t" \
  "v_exp_f32 v133, v133\n\t" \
  "v_exp_f32 v134, v134\n\t" \
  "v_exp_f32 v135, v135\n\t" \
  "v_exp_f32 v136, v136\n\t" \
  "v_exp_f32 v137, v137\n\t" \
  "v_exp_f32 v138, v138\n\t" \
  "v_smfmac_f32_32x32x32_bf16 v[96:111], v[208:211], v[128:135], v242\n\t" \
  "v_exp_f32 v139, v139\n\t" \
  "v_exp_f32 v140, v140\n\t" \
  "v_exp_f32 v141, v141\n\t" \
  "v_exp_f32 v142, v142\n\t" \
  "v_exp_f32 v143, v143\n\t" \
  "v_exp_f32 v112, v112\n\t" \
  "v_exp_f32 v113, v113\n\t" \
  "v_smfmac_f32_32x32x32_bf16 v[96:111], v[212:215], v[136:143], v242\n\t" \
  "s_cbranch_scc1 25f\n\t" \
  "global_load_dwordx4 v[204:207], v240, s[50:51] offset:-2048 sc1\n\t" \
  "s_mov_b64 exec, s[42:43]\n\t" \
  "global_load_dwordx4 v[208:211], v241, s[50:51] offset:-1024 sc1\n\t" \
  "global_load_dwordx4 v[212:215], v241, s[50:51] offset:-512 sc1\n\t" \
  "s_mov_b64 exec, -1\n\t" \
  "15:\n\t" \
  "v_exp_f32 v114, v114\n\t" \
  "v_exp_f32 v115, v115\n\t" \
  "v_exp_f32 v116, v116\n\t" \
  "v_mfma_f32_32x32x16_bf16 v[128:143], v[216:219], v[60:63], v[64:79]\n\t" \
  "v_exp_f32 v117, v117\n\t" \
  "v_exp_f32 v118, v118\n\t" \
  "v_exp_f32 v119, v119\n\t" \
  "v_exp_f32 v120, v120\n\t" \
  "v_exp_f32 v121, v121\n\t" \
  "v_exp_f32 v122, v122\n\t" \
  "v_smfmac_f32_32x32x32_bf16 v[80:95], v[220:223], v[112:119], v242\n\t" \
  "v_exp_f32 v123, v123\n\t" \
  "v_exp_f32 v124, v124\n\t" \
  "v_exp_f32 v125, v125\n\t" \
  "v_exp_f32 v126, v126\n\t" \
  "v_exp_f32 v127, v127\n\t" \
  "v_exp_f32 v128, v128\n\t" \
  "v_exp_f32 v129, v129\n\t" \
  "v_smfmac_f32_32x32x32_bf16 v[80:95], v[224:227], v[120:127], v242\n\t" \
  "v_exp_f32 v130, v130\n\t" \
  "v_exp_f32 v131, v131\n\t" \
  "v_exp_f32 v132, v132\n\t" \
  "s_waitcnt vmcnt(22)\n\t" \
  "v_mfma_f32_32x32x16_bf16 v[112:127], v[228:231], v[56:59], v[64:79]\n\t" \
  "v_exp_f32 v133, v133\n\t" \
  "v_exp_f32 v134, v134\n\t" \
  "v_exp_f32 v135, v135\n\t" \
  "v_exp_f32 v136, v136\n\t" \
  "v_exp_f32 v137, v137\n\t" \
  "v_exp_f32 v138, v138\n\t" \
  "v_smfmac_f32_32x32x32_bf16 v[96:111], v[220:223], v[128:135], v242\n\t" \
  "v_exp_f32 v139, v139\n\t" \
  "v_exp_f32 v140, v140\n\t" \
  "v_exp_f32 v141, v141\n\t" \
  "v_exp_f32 v142, v142\n\t" \
  "v_exp_f32 v143, v143\n\t" \
  "v_exp_f32 v112, v112\n\t" \
  "v_exp_f32 v113, v113\n\t" \
  "v_smfmac_f32_32x32x32_bf16 v[96:111], v[224:227], v[136:143], v242\n\t" \
  "s_cbranch_scc1 26f\n\t" \
  "global_load_dwordx4 v[216:219], v240, s[50:51] offset:0 sc1\n\t" \
  "s_mov_b64 exec, s[42:43]\n\t" \
  "global_load_dwordx4 v[220:223], v241, s[50:51] offset:1024 sc1\n\t" \
  "global_load_dwordx4 v[224:227], v241, s[50:51] offset:1536 sc1\n\t" \
  "s_mov_b64 exec, -1\n\t" \
  "16:\n\t" \
  "v_exp_f32 v114, v114\n\t" \
  "v_exp_f32 v115, v115\n\t" \
  "v_exp_f32 v116, v116\n\t" \
  "v_mfma_f32_32x32x16_bf16 v[128:143], v[228:231], v[60:63], v[64:79]\n\t" \
  "v_exp_f32 v117, v117\n\t" \
  "v_exp_f32 v118, v118\n\t" \
  "v_exp_f32 v119, v119\n\t" \
  "v_exp_f32 v120, v120\n\t" \
  "v_exp_f32 v121, v121\n\t" \
  "v_exp_f32 v122, v122\n\t" \
  "v_smfmac_f32_32x32x32_bf16 v[80:95], v[232:235], v[112:119], v242\n\t" \
  "v_exp_f32 v123, v123\n\t" \
  "v_exp_f32 v124, v124\n\t" \
  "v_exp_f32 v125, v125\n\t" \
  "v_exp_f32 v126, v126\n\t" \
  "v_exp_f32 v127, v127\n\t" \
  "v_exp_f32 v128, v128\n\t" \
  "v_exp_f32 v129, v129\n\t" \
  "v_smfmac_f32_32x32x32_bf16 v[80:95], v[236:239], v[120:127], v242\n\t" \
  "v_exp_f32 v130, v130\n\t" \
  "v_exp_f32 v131, v131\n\t" \
  "v_exp_f32 v132, v132\n\t" \
  "s_waitcnt vmcnt(18)\n\t" \
  "v_mfma_f32_32x32x16_bf16 v[112:127], v[144:147], v[56:59], v[64:79]\n\t" \
  "v_exp_f32 v133, v133\n\t" \
  "v_exp_f32 v134, v134\n\t" \
  "v_exp_f32 v135, v135\n\t" \
  "v_exp_f32 v136, v136\n\t" \
  "v_exp_f32 v137, v137\n\t" \
  "v_exp_f32 v138, v138\n\t" \
  "v_smfmac_f32_32x32x32_bf16 v[96:111], v[232:235], v[128:135], v242\n\t" \
  "v_exp_f32 v139, v139\n\t" \
  "v_exp_f32 v140, v140\n\t" \
  "v_exp_f32 v141, v141\n\t" \
  "v_exp_f32 v142, v142\n\t" \
  "v_exp_f32 v143, v143\n\t" \
  "v_exp_f32 v112, v112\n\t" \
  "v_exp_f32 v113, v113\n\t" \
  "v_smfmac_f32_32x32x32_bf16 v[96:111], v[236:239], v[136:143], v242\n\t" \
  "s_cbranch_scc1 27f\n\t" \
  "global_load_dwordx4 v[228:231], v240, s[50:51] offset:2048 sc1\n\t" \
  "s_mov_b64 exec, s[42:43]\n\t" \
  "global_load_dwordx4 v[232:235], v241, s[50:51] offset:3072 sc1\n\t" \
  "global_load_dwordx4 v[236:239], v241, s[50:51] offset:3584 sc1\n\t" \
  "s_mov_b64 exec, -1\n\t" \
  "17:\n\t" \
  "s_add_u32 s40, s40, 16384\n\t" \
  "s_addc_u32 s41, s41, 0\n\t" \
  "s_add_u32 s50, s50, 16384\n\t" \
  "s_addc_u32 s51, s51, 0\n\t" \
  "s_add_u32 m0, m0, 4096\n\t" \
  "s_add_u32 s46, s46, 16384\n\t" \
  "s_addc_u32 s47, s47, 0\n\t" \
  "s_sub_u32 s44, s44, 1\n\t" \
  "s_cmp_eq_u32 s44, 1\n\t" \
  "s_branch 1b\n\t" \
  "20:\n\t" \
  "s_waitcnt vmcnt(19)\n\t" \
  "s_branch 10b\n\t" \
  "21:\n\t" \
  "s_waitcnt vmcnt(16)\n\t" \
  "s_branch 11b\n\t" \
  "22:\n\t" \
  "s_waitcnt vmcnt(13)\n\t" \
  "s_branch 12b\n\t" \
  "23:\n\t" \
  "s_waitcnt vmcnt(10)\n\t" \
  "s_branch 13b\n\t" \
  "24:\n\t" \
  "s_waitcnt vmcnt(7)\n\t" \
  "s_branch 14b\n\t" \
  "25:\n\t" \
  "s_waitcnt vmcnt(4)\n\t" \
  "s_branch 15b\n\t" \
  "26:\n\t" \
  "s_branch 16b\n\t" \
  "27:\n\t" \
  "s_nop 15\n\t"
#define CLOB "v64", "v65", "v66", "v67", "v68", "v69", "v70", "v71", "v72", "v73", "v74", "v75", "v76", "v77", "v78", "v79", "v112", "v113", "v114", "v115", "v116", "v117", "v118", "v119", "v120", "v121", "v122", "v123", "v124", "v125", "v126", "v127", "v128", "v129", "v130", "v131", "v132", "v133", "v134", "v135", "v136", "v137", "v138", "v139", "v140", "v141", "v142", "v143", "v144", "v145", "v146", "v147", "v148", "v149", "v150", "v151", "v152", "v153", "v154", "v155", "v156", "v157", "v158", "v159", "v160", "v161", "v162", "v163", "v164", "v165", "v166", "v167", "v168", "v169", "v170", "v171", "v172", "v173", "v174", "v175", "v176", "v177", "v178", "v179", "v180", "v181", "v182", "v183", "v184", "v185", "v186", "v187", "v188", "v189", "v190", "v191", "v192", "v193", "v194", "v195", "v196", "v197", "v198", "v199", "v200", "v201", "v202", "v203", "v204", "v205", "v206", "v207", "v208", "v209", "v210", "v211", "v212", "v213", "v214", "v215", "v216", "v217", "v218", "v219", "v220", "v221", "v222", "v223", "v224", "v225", "v226", "v227", "v228", "v229", "v230", "v231", "v232", "v233", "v234", "v235", "v236", "v237", "v238", "v239"

#define MAP 1
#define ROT (qb & 7)
#define SS 2
#define KGS 8, 10, 12, 14, 16, 18, 20, 22
constexpr int KGU[9] = {KGS, 32};
#define QF1 3
#define U_STEP 7
#define NREG 8
#define F0_STEP 1
#define PRIO_Q 1

constexpr unsigned WS_F0 = 0;
constexpr unsigned WS_F1 = WS_F0 + 32768;
constexpr unsigned WS_F2 = WS_F1 + 32768;
constexpr unsigned KV_HEAD = 64 * 2048;
constexpr unsigned WS_KV = 1u << 20;
constexpr unsigned WS_UT = 5u << 20;
constexpr unsigned WS_YT = 7u << 20;
constexpr unsigned WS_O = 9u << 20;
constexpr unsigned WS_AREA = 11u << 20;
constexpr unsigned WS_A0 = 1u << 20;
constexpr unsigned WS_DBG = 512u << 10;
constexpr unsigned L_SLOT = 24576;
constexpr unsigned L_VA = 98304;
constexpr unsigned L_KF = L_VA + 4 * 8448;
constexpr unsigned L_OV = L_KF + 8320;
constexpr unsigned L_FL = L_OV + 288;
constexpr unsigned L_PR = L_FL + 112;
constexpr unsigned L_QF = L_PR + 4096;
constexpr unsigned L_EX = L_QF + 8192;
constexpr unsigned L_S = 131072;
constexpr unsigned L_END = 163840;
static_assert(L_EX + 10240 <= L_END && L_PR % 16 == 0, "lds");
constexpr unsigned FL_CONV = L_FL, FL_C2 = L_FL + 16, FL_C1 = L_FL + 20, FL_WQ = L_FL + 32, FL_Q = L_FL + 48, FL_PART = L_FL + 64, FL_TAB = L_FL + 80, FL_WQ2 = L_FL + 96;

DEV float b2f(unsigned x) { return __uint_as_float(x << 16); }
DEV unsigned pk(float a, float b) { f32x2 v = {a, b}; return __builtin_bit_cast(unsigned, __builtin_convertvector(v, bf16x2)); }
DEV f32x16 mfma(u32x4 a, u32x4 b, f32x16 c) {
  return __builtin_amdgcn_mfma_f32_32x32x16_bf16(__builtin_bit_cast(bf16x8, a), __builtin_bit_cast(bf16x8, b), c, 0, 0, 0);
}
DEV bool allv(bool c) { return __builtin_amdgcn_ballot_w64(!c) == 0; }
DEV float maxh(float m) {
  auto rr = __builtin_amdgcn_permlane32_swap(__float_as_uint(m), __float_as_uint(m), false, false);
  return fmaxf(__uint_as_float(rr[0]), __uint_as_float(rr[1]));
}
typedef AS3 char* lds_t;
DEV lds_t lb(unsigned off) { asm("" : "+v"(off)); return (lds_t)(size_t)off; }
template <class T> DEV AS3 T* lp(unsigned off) { return (AS3 T*)(size_t)(off); }
template <class T> DEV AS3 T* lp(lds_t p) { return (AS3 T*)p; }
DEV u32x2 trd(lds_t p) { return __builtin_bit_cast(u32x2, __builtin_amdgcn_ds_read_tr16_b64_v4i16((AS3 s16x4*)p)); }
DEV u32x4 trd2(lds_t a, lds_t b) { u32x2 x = trd(a), y = trd(b); return u32x4{x[0], x[1], y[0], y[1]}; }
DEV void dma(const char* sbase, unsigned voff, unsigned dst) {
  asm volatile("s_mov_b32 m0, %2\n\ts_nop 0\n\tglobal_load_lds_dwordx4 %0, %1" ::"v"(voff), "s"(sbase), "s"(dst) : "memory");
}
#define BAR() asm volatile("s_waitcnt lgkmcnt(0)\n\ts_barrier" ::: "memory")
#define VMW(n) asm volatile("s_waitcnt vmcnt(" #n ")" ::: "memory")
#define VMBAR(n) asm volatile("s_waitcnt vmcnt(" #n ") lgkmcnt(0)\n\ts_barrier" ::: "memory")
#define CFENCE() asm volatile("" ::: "memory")
#define SC1 16
#define ST(i)
#define ATT_ST
#define ATT_STO
#define ATT_STC
constexpr unsigned SPIN_CAP = 1u << 16;

DEV void poll(const char* p, u64 tag) {
#pragma unroll 1
  for (unsigned s = 0; s < SPIN_CAP; ++s) {
    u64 v = __hip_atomic_load((const AS1 u64*)p, __ATOMIC_RELAXED, __HIP_MEMORY_SCOPE_AGENT);
    if (allv(v == tag)) break;
    __builtin_amdgcn_s_sleep(1);
  }
  CFENCE();
}

#define SBAR() __builtin_amdgcn_sched_barrier(0)
DEV void lset(unsigned off) {
  asm volatile("s_waitcnt lgkmcnt(0)" ::: "memory");
  *(volatile AS3 unsigned*)(size_t)off = 1;
}
DEV void lwait(unsigned off) {
#pragma unroll 1
  for (unsigned s = 0; s < SPIN_CAP; ++s) {
    if (allv(*(volatile AS3 unsigned*)(size_t)off != 0)) break;
    __builtin_amdgcn_s_sleep(1);
  }
  CFENCE();
}
DEV bool lcount(unsigned off, unsigned last, int lane) {
  unsigned old = 0;
  if (lane == 0) old = __hip_atomic_fetch_add(lp<unsigned>(off), 1u, __ATOMIC_RELAXED, __HIP_MEMORY_SCOPE_WORKGROUP);
  return __builtin_amdgcn_readfirstlane(old) == last;
}

DEV unsigned kg_fin(float ex, float th) {
  th = __builtin_amdgcn_fractf(th);
  float m = __builtin_amdgcn_exp2f(ex);
  return pk(m * __builtin_amdgcn_cosf(th), m * __builtin_amdgcn_sinf(th));
}
DEV unsigned kg_a(f32x4 p, float a) { return kg_fin(__builtin_fmaf(p[0], a, p[2]), __builtin_fmaf(p[1], a, p[3])); }
DEV unsigned kg_b(f32x4 p, float a) { return kg_fin(p[0] * a, -(p[1] * a)); }

template <int SET> DEV void conv_wave(unsigned fa, unsigned ga, f32x16 (&acc)[4]) {
  constexpr int T[4] = {SET ? 6 : 7, SET ? 5 : 4, SET ? 2 : 3, SET ? 1 : 0};
  u32x4 F[8], G[8];
  {
    lds_t f = lb(fa), g = lb(ga);
#pragma unroll
    for (int a = 0; a <= T[0]; ++a) G[a] = *lp<u32x4>(g + 512 * a);
#pragma unroll
    for (int a = 0; a <= T[0]; ++a) F[a] = trd2(f + 512 * a + 32, f + 512 * a);
  }
#pragma unroll 1
  for (int rho = 0; rho < 4; ++rho) {
    fa += 128; ga -= 128;
    lds_t f = lb(fa), g = lb(ga);
#pragma unroll
    for (int a = 0; a <= T[0]; ++a) {
      SBAR();
#pragma unroll
      for (int k = 0; k < 4; ++k)
        if (a <= T[k]) acc[k] = mfma(G[T[k] - a], F[a], acc[k]);
      SBAR();
      G[T[0] - a] = *lp<u32x4>(g + 512 * (T[0] - a));
      F[a] = trd2(f + 512 * a + 32, f + 512 * a);
    }
  }
}

#ifdef __HIP_DEVICE_COMPILE__
#define NOPK __attribute__((target("no-packed-fp32-ops")))
#else
#define NOPK
#endif
__global__ NOPK void __launch_bounds__(512, 2)
fused(const char* __restrict__ u, const u16* __restrict__ Are, const u16* __restrict__ Aim, const u16* __restrict__ Bre,
      const u16* __restrict__ Bim, const u16* __restrict__ Cre, const u16* __restrict__ Cim, const u16* __restrict__ ldt,
      const char* __restrict__ Dsk, const char* __restrict__ x, const char* __restrict__ wqkv, const char* __restrict__ wo,
      char* __restrict__ out, char* __restrict__ ws) {
  __shared__ __attribute__((aligned(16))) char lds_all[L_END];
  asm volatile("" ::"v"(lds_all), "s"(u), "s"(Are), "s"(Aim), "s"(Bre), "s"(Bim), "s"(Cre), "s"(Cim), "s"(ldt), "s"(Dsk), "s"(x), "s"(wqkv), "s"(wo), "s"(out), "s"(ws));
  const int b = blockIdx.x;
  const int hq = (b & 1) * 4 + ((b >> 3) & 3), qb = ((b >> 1) & 3) * 8 + (b >> 5);
  const int lane = threadIdx.x & 63, w = __builtin_amdgcn_readfirstlane(threadIdx.x >> 6);
  const int r = lane & 31, h = lane >> 5;
  const int q4 = (lane >> 2) & 3, pp = lane & 3, g1 = (lane >> 4) & 1;
  const int p = w & 1, t = (w >> 1) & 1;
  ST(0);
  const lds_t wta = lb((8 * h + q4) * 128 + ((64 * p + 32 * g1 + 8 * pp) ^ (64 * (q4 >> 1))));
  u64 tag;
  unsigned epoch;
  char* wa;
  __amdgpu_buffer_rsrc_t wsr;
  auto mktag = [&](unsigned e0) __attribute__((always_inline)) {
    epoch = __builtin_amdgcn_readfirstlane(e0) * 1664525u + 1013904223u;
    tag = ((u64)(epoch * 0x9E3779B1u + 0x7F4A7C15u) << 32) | epoch;
    wa = ws + WS_A0 + (epoch & (NREG - 1)) * WS_AREA;
    wsr = __builtin_amdgcn_make_buffer_rsrc(wa, 0, WS_AREA, 0x00020000);
  };
  auto raise = [&](unsigned f) __attribute__((always_inline)) {
    if (lane == 0) __hip_atomic_store((AS1 u64*)(wa + f + (hq * 32 + qb) * 128), tag, __ATOMIC_RELAXED, __HIP_MEMORY_SCOPE_AGENT);
  };
  u32x4 q0, q1;

  if (w >= 4) {
    const int kw = w - 4, rot = ROT;
    unsigned e0;
    asm volatile("global_load_dword %0, %1, %2 sc1" : "=v"(e0) : "v"(0u), "s"(ws) : "memory");
    const unsigned row = 16 * kw + (lane >> 3);
    const unsigned xo0 = row * 1024 + (((lane & 7) ^ (lane >> 4)) << 4);
    const unsigned xo1 = (row + 8) * 1024 + (((lane & 7) ^ (lane >> 4) ^ 4) << 4);
    const unsigned wo0 = row * 3072 + (((lane & 7) ^ ((lane >> 2) & 4)) << 4);
    const unsigned wo1 = wo0 + 8 * 3072;
    const char* xb = x + qb * 65536;
    const char* wb = wqkv + hq * 128;
    const unsigned ld = kw * 2048;
    auto DW = [&](int c, int seg, unsigned dst) __attribute__((always_inline)) { const char* s = wb + ((c + rot) & 7) * (64 * 3072) + seg * 1024; dma(s, wo0, dst + ld); dma(s, wo1, dst + ld + 1024); };
    auto DC = [&](int c) __attribute__((always_inline)) {
      const unsigned d = (c & 3) * L_SLOT;
      const char* s = xb + (((c + rot) & 7) << 7);
      dma(s, xo0, d + ld); dma(s, xo1, d + ld + 1024);
      DW(c, 1, d + 8192); DW(c, 2, d + 16384);
    };
    auto D1 = [&](int c, int i) __attribute__((always_inline)) {
      const unsigned d = (c & 3) * L_SLOT + (i >> 1) * 8192 + (i & 1) * 1024 + ld;
      const char* s = i < 2 ? xb + (((c + rot) & 7) << 7) : wb + ((c + rot) & 7) * (64 * 3072) + (i >> 1) * 1024;
      dma(s, i < 2 ? (i ? xo1 : xo0) : (i & 1 ? wo1 : wo0), d);
      asm volatile("s_nop 1");
    };
    auto DQ = [&](int i) __attribute__((always_inline)) {
      dma(wb + (((i >> 1) + rot) & 7) * (64 * 3072), i & 1 ? wo1 : wo0, (i >> 1) * 8192 + (i & 1) * 1024 + ld);
      asm volatile("s_nop 1");
    };
    ST(14);
    DC(0); DC(1); DC(2);
    ST(1);
    const int rs = (r & 19) | ((r & 4) << 1) | ((r & 8) >> 1);
    const unsigned xrow = 32 * t + rs;
    const unsigned xa = xrow * 128 + ((h ^ ((xrow >> 1) & 7)) << 4);
    const lds_t xs[4] = {lb(xa), lb(xa ^ 32), lb(xa ^ 64), lb(xa ^ 96)};
    const lds_t xs2[4] = {lb(xa + 2 * L_SLOT), lb((xa ^ 32) + 2 * L_SLOT), lb((xa ^ 64) + 2 * L_SLOT), lb((xa ^ 96) + 2 * L_SLOT)};
    const lds_t wt2 = lb((unsigned)(size_t)wta + 2 * L_SLOT);
    u32x4 xr[32];
    f32x16 acc = {0}, acv = {0};
    __builtin_amdgcn_s_setprio(PRIO_Q);
#pragma unroll
    for (int c = 0; c < 8; ++c) {
      if (c == SS + 1) { ST(16); }
      VMBAR(12);
      if (c == 0) ST(18);
      if (c == SS) ST(15);
      if (c == SS + 1) ST(17);
      if (c == 7) ST(19);
      u32x4 wk[4], wv[4];
      auto RD = [&](int s) __attribute__((always_inline)) {
        xr[4 * c + s] = *lp<u32x4>(((c & 2) ? xs2[s] : xs[s]) + (c & 1) * L_SLOT);
        lds_t a = ((c & 2) ? wt2 : wta) + (c & 1) * L_SLOT + 8192 + s * 2048;
        wk[s] = trd2(a, a + 512);
        SBAR();
        wv[s] = trd2(a + 8192, a + 8704);
        SBAR();
      };
      auto GAP = [&](int i) __attribute__((always_inline)) {
        SBAR();
        if (i == 0) RD(2);
        else if (i == 1) RD(3);
        else if (c < 5) D1(c + 3, i - 2);
        else if (c < 7) DQ(6 * (c - 5) + i - 2);
        SBAR();
      };
      RD(0); RD(1);
#pragma unroll
      for (int s = 0; s < 4; ++s) {
        acc = mfma(wk[s], xr[4 * c + s], acc);
        GAP(2 * s);
        acv = mfma(xr[4 * c + s], wv[s], acv);
        GAP(2 * s + 1);
      }
      if (c == 5) { asm volatile("" : "+v"(e0)); mktag(e0); }
    }
    {
      unsigned vo = WS_KV + (4 * hq + 2 * p) * KV_HEAD + (2 * qb + t) * 2048;
      unsigned vk = vo + (rs + 32 * h) * 16, vv = vo + g1 * KV_HEAD + 1024 + h * 256 + (lane & 15) * 16;
      __builtin_amdgcn_raw_buffer_store_b128(u32x4{pk(acc[0], acc[1]), pk(acc[2], acc[3]), pk(acc[4], acc[5]), pk(acc[6], acc[7])}, wsr, vk, 0, SC1);
      __builtin_amdgcn_raw_buffer_store_b128(u32x4{pk(acc[8], acc[9]), pk(acc[10], acc[11]), pk(acc[12], acc[13]), pk(acc[14], acc[15])}, wsr, vk, KV_HEAD, SC1);
      __builtin_amdgcn_raw_buffer_store_b128(u32x4{pk(acv[0], acv[1]), pk(acv[2], acv[3]), pk(acv[4], acv[5]), pk(acv[6], acv[7])}, wsr, vv, 0, SC1);
      __builtin_amdgcn_raw_buffer_store_b128(u32x4{pk(acv[8], acv[9]), pk(acv[10], acv[11]), pk(acv[12], acv[13]), pk(acv[14], acv[15])}, wsr, vv + 512, 0, SC1);
    }
    ST(2);
    DW(6, 0, 6 * 8192); DW(7, 0, 7 * 8192);
    VMW(8);
    lset(FL_WQ + kw * 4);
    lwait(FL_WQ + pp * 4);
    ST(6);
    acc = f32x16{0}; acv = f32x16{0};
    {
      u32x4 wf[2][4];
      auto RQ = [&](int c) __attribute__((always_inline)) {
#pragma unroll
        for (int s = 0; s < 4; ++s) { lds_t a = wta + c * 8192 + s * 2048; wf[c & 1][s] = trd2(a, a + 512); }
      };
      RQ(0);
#pragma unroll
      for (int c = 0; c < 8; ++c) {
        SBAR();
        if (c == QF1) {
          VMW(4);
          ST(3);
          if (lcount(FL_C1, 3, lane)) { raise(WS_F1); VMW(1); } else VMW(0);
          lset(FL_WQ2 + kw * 4);
        }
        if (c == 5) lwait(FL_WQ2 + pp * 4);
        if (c < 7) RQ(c + 1);
        SBAR();
#pragma unroll
        for (int s = 0; s < 4; s += 2) { acc = mfma(wf[c & 1][s], xr[4 * c + s], acc); acv = mfma(wf[c & 1][s + 1], xr[4 * c + s + 1], acv); }
      }
    }
    acc += acv;
    const float qs = 0.25f * 1.44269504088896341f;
    acc *= qs;
    lds_t qa = lb(L_QF + (2 * p) * 2048 + t * 1024 + (rs + 32 * h) * 16);
    *lp<u32x4>(qa) = u32x4{pk(acc[0], acc[1]), pk(acc[2], acc[3]), pk(acc[4], acc[5]), pk(acc[6], acc[7])};
    *lp<u32x4>(qa + 2048) = u32x4{pk(acc[8], acc[9]), pk(acc[10], acc[11]), pk(acc[12], acc[13]), pk(acc[14], acc[15])};
    lset(FL_Q + kw * 4);
    ST(4);
    const char* f1p = wa + WS_F1 + (hq * 32 + (lane & 15)) * 128;
    u64 f1v = __hip_atomic_load((const AS1 u64*)f1p, __ATOMIC_RELAXED, __HIP_MEMORY_SCOPE_AGENT);
    lwait(FL_Q + pp * 4);
    q0 = *lp<u32x4>(L_QF + (w & 3) * 2048 + lane * 16);
    q1 = *lp<u32x4>(L_QF + (w & 3) * 2048 + 1024 + lane * 16);
    ST(5);
    if (!allv(f1v == tag)) poll(f1p, tag);
    CFENCE();
    __builtin_amdgcn_s_setprio(0);
  } else {
    const int cl = w >> 1, mh = w & 1, ch = hq * 64 + 2 * qb + cl;
    const unsigned pi = (ch * 128 + mh * 64 + lane) * 2;
    unsigned e0, l_dt, l_ar, l_ai, l_br, l_bi, l_cr, l_ci;
    u32x4 ua, uc;
    asm volatile("global_load_ushort %0, %7, %8\n\tglobal_load_ushort %1, %7, %9\n\tglobal_load_ushort %2, %7, %10\n\t"
                 "global_load_ushort %3, %7, %11\n\tglobal_load_ushort %4, %7, %12\n\tglobal_load_ushort %5, %7, %13\n\t"
                 "global_load_ushort %6, %14, %15"
                 : "=&v"(l_br), "=&v"(l_bi), "=&v"(l_cr), "=&v"(l_ci), "=&v"(l_ar), "=&v"(l_ai), "=&v"(l_dt)
                 : "v"(pi), "s"(Bre), "s"(Bim), "s"(Cre), "s"(Cim), "s"(Are), "s"(Aim), "v"(ch * 2), "s"(ldt) : "memory");
    asm volatile("global_load_dword %0, %3, %4 sc1\n\tglobal_load_dwordx4 %1, %5, %6\n\tglobal_load_dwordx4 %2, %5, %7"
                 : "=&v"(e0), "=&v"(ua), "=&v"(uc)
                 : "v"(0u), "s"(ws), "v"((lane >> 3) * 4096 + (lane & 7) * 16), "s"(u + (qb * 64 + w) * 1024 + hq * 128),
                   "s"(u + (qb * 64 + 32 + w) * 1024 + hq * 128) : "memory");
    float aA, aB = (float)(8 * r);
    {
      int e = r & 3, g = r >> 2;
      int v = g < 2 ? r : g < 4 ? 256 + 4 * (g - 2) + (e ? 2 * (e - 1) : 0) : g < 6 ? 512 + 256 * (g - 4) + 4 * (e < 2 ? e : 2) : 1024 + 512 * (g - 6) + 256 * (e >> 1) + 8 * (e & 1);
      aA = (float)v;
    }
    const unsigned yb = L_VA + w * 8448, up = L_EX + cl * 4608, ul = yb + 4608;
    if (w == 0 && lane < 28) *lp<unsigned>(L_FL + lane * 4) = 0;
    if (lane < 4) { *lp<u32x4>(L_KF + cl * 4160 + lane * 16) = u32x4{0}; *lp<unsigned>(L_OV + cl * 144 + lane * 4) = 0; }
    if (lane < 32) *lp<u32x4>(up + lane * 16) = u32x4{0};
    const lds_t pr = lb(L_PR + w * 1024 + h * 64), pw = lb(L_PR + w * 1024 + lane * 16);
    const lds_t uw0 = lb(ul + lane * 16), ur0 = lb(ul + q4 * 128 + (lane >> 4) * 32 + pp * 8);
    asm volatile("s_waitcnt vmcnt(3)" : "+v"(l_br), "+v"(l_bi), "+v"(l_cr), "+v"(l_ci), "+v"(l_ar), "+v"(l_ai), "+v"(l_dt), "+v"(aA), "+v"(aB)::"memory");
    ST(14);
    {
      float dt = b2f(pk(__expf(b2f(l_dt)), 0.f));
      float br = b2f(l_br) * dt, bi = b2f(l_bi) * dt, cr = b2f(l_cr), ci = b2f(l_ci);
      float wr = cr * br - ci * bi, wi = cr * bi + ci * br;
      float mx = fmaxf(fabsf(wr), fabsf(wi)), mn = fminf(fabsf(wr), fabsf(wi));
      int ex = __builtin_amdgcn_frexp_expf(mx);
      float rt = __builtin_amdgcn_ldexpf(mn, -ex) * __builtin_amdgcn_rcpf(__builtin_amdgcn_frexp_mantf(mx));
      rt = mx > 0.f ? rt : 0.f;
      float r2 = rt * rt;
      float at = __builtin_fmaf(r2, 0x1.5a54b0p-9f, -0x1.f4b218p-7f);
      at = __builtin_fmaf(r2, at, 0x1.53f67ep-5f);
      at = __builtin_fmaf(r2, at, -0x1.2fa9aep-4f);
      at = __builtin_fmaf(r2, at, 0x1.b26364p-4f);
      at = __builtin_fmaf(r2, at, -0x1.22c1ccp-3f);
      at = __builtin_fmaf(r2, at, 0x1.99717ep-3f);
      at = __builtin_fmaf(r2, at, -0x1.5554c4p-2f);
      at = __builtin_fmaf(rt, r2 * at, rt) * 0.15915494309189535f;
      at = fabsf(wi) > fabsf(wr) ? 0.25f - at : at;
      at = wr < 0.f ? 0.5f - at : at;
      f32x4 pv;
      pv[0] = fmaxf(dt * b2f(l_ar) * 1.44269504088896341f, -1e30f);
      pv[1] = dt * b2f(l_ai) * 0.15915494309189535f;
      pv[2] = __log2f(mx) + 0.5f * __log2f(1.f + r2);
      pv[3] = __builtin_copysignf(at, wi);
      *lp<f32x4>(pw) = pv;
    }
    f32x4 pm[32];
#pragma unroll
    for (int e = 0; e < KGU[0]; ++e) pm[e] = *lp<f32x4>(pr + (e >> 2) * 128 + (e & 3) * 16);
    asm volatile("s_waitcnt vmcnt(0)" : "+v"(e0), "+v"(ua), "+v"(uc)::"memory");
    mktag(e0);
    {
      *lp<u32x4>(uw0) = ua;
      *lp<u32x4>(uw0 + 1024) = uc;
      u32x4 t0 = trd2(ur0, ur0 + 512), t1 = trd2(ur0 + 1024, ur0 + 1536);
      unsigned vo = WS_UT + ((hq * 32 + qb) * 8 + 2 * w) * 1024 + lane * 16;
      __builtin_amdgcn_raw_buffer_store_b128(t0, wsr, vo, 0, SC1);
      __builtin_amdgcn_raw_buffer_store_b128(t1, wsr, vo + 1024, 0, SC1);
    }
    f32x16 kacc = {0};
    ST(1);
    const char* f0p = wa + WS_F0 + (hq * 32 + 8 * w + (lane & 7)) * 128;
    u64 f0v;
    u32x4 uv[2], fa, fb;
    bool early;
    const unsigned uo = WS_UT + (hq * 256 + 64 * w + (lane >> 1)) * 1024 + (2 * qb + (lane & 1)) * 16;
#pragma unroll
    for (int n = 0; n < 32; ++n) {
      int c = 0;
      while (c < 8 && KGU[c] < n + 1) ++c;
      const bool last = c < 8 && KGU[c] == n + 1;
      if (last) {
#pragma unroll
        for (int e = n + 1; e < KGU[c + 1]; ++e) pm[e] = *lp<f32x4>(pr + (e >> 2) * 128 + (e & 3) * 16);
      }
      SBAR();
      fa[n & 3] = kg_a(pm[n], aA);
      fb[n & 3] = kg_b(pm[n], aB);
      if ((n & 3) == 3) kacc = mfma(fa, fb, kacc);
      if (n == 31) {
        if (mh) { *lp<f32x4>(uw0) = f32x4{kacc[0], kacc[1], kacc[2], kacc[3]}; *lp<f32x4>(uw0 + 1024) = f32x4{kacc[4], kacc[5], kacc[6], kacc[7]}; }
        else { *lp<f32x4>(uw0) = f32x4{kacc[8], kacc[9], kacc[10], kacc[11]}; *lp<f32x4>(uw0 + 1024) = f32x4{kacc[12], kacc[13], kacc[14], kacc[15]}; }
      }
      SBAR();
      if (last) {
        if (c == SS + 1) ST(16);
        if (c == F0_STEP) { VMBAR(0); if (w == 0) raise(WS_F0); } else BAR();
        if (c == SS) ST(15);
        if (c == SS + 1) ST(17);
        if (c == U_STEP - 2) f0v = __hip_atomic_load((const AS1 u64*)f0p, __ATOMIC_RELAXED, __HIP_MEMORY_SCOPE_AGENT);
        if (c == U_STEP) {
          early = allv(f0v == tag);
          CFENCE();
          if (early) {
            uv[0] = __builtin_amdgcn_raw_buffer_load_b128(wsr, uo, 0, SC1);
            uv[1] = __builtin_amdgcn_raw_buffer_load_b128(wsr, uo, 32768, SC1);
          }
        }
        if (c == 7) {
          ST(2);
          if (!early) {
            poll(f0p, tag);
            uv[0] = __builtin_amdgcn_raw_buffer_load_b128(wsr, uo, 0, SC1);
            uv[1] = __builtin_amdgcn_raw_buffer_load_b128(wsr, uo, 32768, SC1);
            ST(3);
          }
        }
      }
    }
    if (KGU[7] < 32) { lset(FL_PART + w * 4); lwait(FL_PART + (w ^ 1) * 4); }
    ST(4);
    {
      lds_t pa = lb(L_VA + (w ^ 1) * 8448 + 4608 + lane * 16);
      f32x4 v0 = *lp<f32x4>(pa), v1 = *lp<f32x4>(pa + 1024);
      unsigned kf = L_KF + cl * 4160 + 64;
      if (!mh) {
        *lp<u32x2>(kf + r * 16 + h * 8) = u32x2{pk(v0[0] + kacc[0], v0[1] + kacc[1]), pk(v0[2] + kacc[2], v0[3] + kacc[3])};
        *lp<u32x2>(kf + 512 + r * 16 + h * 8) = u32x2{pk(v1[0] + kacc[4], v1[1] + kacc[5]), pk(v1[2] + kacc[6], v1[3] + kacc[7])};
      } else {
        float a0 = v0[0] + kacc[8], a4 = v0[1] + kacc[9], a8 = v0[2] + kacc[10];
        *lp<u32x4>(kf + 1024 + h * 512 + r * 16) = u32x4{pk(a0, a0), pk(a0, a4), pk(a4, a4), pk(a8, a8)};
        float c0 = v1[0] + kacc[12], c8 = v1[1] + kacc[13], d0 = v1[2] + kacc[14], d8 = v1[3] + kacc[15];
        bool odd = r & 1;
        *lp<u32x4>(kf + 2048 + h * 1024 + r * 16) = u32x4{pk(c0, c0), pk(c0, c0), pk(odd ? c8 : c0, c8), pk(c8, c8)};
        *lp<u32x4>(kf + 2560 + h * 1024 + r * 16) = u32x4{pk(d0, d0), pk(d0, d0), pk(odd ? d8 : d0, d8), pk(d8, d8)};
      }
    }
    {
      lds_t uw = lb(L_EX + (lane & 1) * 4608 + 512 + (64 * w + (lane >> 1)) * 16);
      *lp<u32x4>(uw) = uv[0];
      *lp<u32x4>(uw + 512) = uv[1];
    }
    lset(FL_TAB + w * 4);
    ST(5);
    lwait(FL_TAB + pp * 4);
    ST(6);
    {
      const unsigned fa0 = L_KF + cl * 4160 + 128 + 32 * g1 + 8 * pp - 64 * h - 32 - 8 * q4;
      const int qq = (r >> 2) - 1 + h;
      const unsigned ga0 = up + 128 * (4 + (qq >> 1)) + 32 * (r & 3) + 16 * (qq & 1);
      f32x16 ya[4];
#pragma unroll
      for (int k = 0; k < 4; ++k) ya[k] = f32x16{0};
      if (mh) conv_wave<1>(fa0, ga0, ya); else conv_wave<0>(fa0, ga0, ya);
      ST(18);
      const unsigned tiles = mh ? 0x01020506u : 0x00030407u;
      f32x4 y[4];
      lds_t yw = lb(yb + lane * 4), yr = lb(yb + h * 4160 + r * 32), yo = lb(yb + (lane >> 2) * 4160 + 1024 + pp * 4);
#pragma unroll
      for (int i = 0; i < 2; ++i) {
        CFENCE();
        if (lane < 16) *lp<u32x4>(yb + (lane >> 1) * 1040 + (lane & 1) * 1024) = u32x4{0};
        CFENCE();
#pragma unroll
        for (int k = 0; k < 2; ++k)
#pragma unroll
          for (int j = 0; j < 16; ++j) *lp<float>(yw + k * 4160 + (j & 3) * 1044 + (j >> 2) * 256) = ya[2 * i + k][j];
        CFENCE();
        y[2 * i] = (*lp<f32x4>(yr) + *lp<f32x4>(yr + 1040)) + (*lp<f32x4>(yr + 2080) + *lp<f32x4>(yr + 3120));
        y[2 * i + 1] = (*lp<f32x4>(yr + 16) + *lp<f32x4>(yr + 1056)) + (*lp<f32x4>(yr + 2096) + *lp<f32x4>(yr + 3136));
        if (lane < 8)
          *lp<float>(L_OV + cl * 144 + (((tiles >> (16 * i + 8 * (lane >> 2))) & 7) + 1) * 16 + pp * 4) =
              *lp<float>(yo + 1040) + *lp<float>(yo + 2080) + *lp<float>(yo + 3120);
        CFENCE();
      }
      lwait(FL_Q + pp * 4);
      q0 = *lp<u32x4>(L_QF + w * 2048 + lane * 16);
      q1 = *lp<u32x4>(L_QF + w * 2048 + 1024 + lane * 16);
      lset(FL_CONV + w * 4);
      {
        unsigned vo = WS_O + (2 * qb * 32 + 4 * hq + w) * 1024 + lane * 16;
        u32x4 ones = {~0u, ~0u, ~0u, ~0u};
        __builtin_amdgcn_raw_buffer_store_b128(ones, wsr, vo, 0, SC1);
        __builtin_amdgcn_raw_buffer_store_b128(ones, wsr, vo, 32768, SC1);
      }
      lwait(FL_CONV + pp * 4);
#pragma unroll
      for (int i = 0; i < 2; ++i) {
        int tl = (tiles >> (16 * i + 8 * h)) & 7;
        f32x4 y0 = y[2 * i] + *lp<f32x4>(L_OV + cl * 144 + (r ? 0 : tl) * 16), y1 = y[2 * i + 1];
        __builtin_amdgcn_raw_buffer_store_b128(u32x4{pk(y0[0], y0[1]), pk(y0[2], y0[3]), pk(y1[0], y1[1]), pk(y1[2], y1[3])}, wsr,
                                               WS_YT + ch * 4096 + tl * 512 + r * 16, 0, SC1);
      }
    }
    ST(19);
    poll(wa + WS_F1 + (hq * 32 + 16 + (lane & 15)) * 128, tag);
    VMW(0);
    if (lcount(FL_C2, 3, lane)) raise(WS_F2);
  }
  ST(7);

  const int head = 4 * hq + (w & 3);
  const unsigned kvo = WS_KV + head * KV_HEAD + (w < 4 ? 32 * 2048 : 0);
  const unsigned koff = lane * 16, voff = (lane & 15) * 16 + h * 256;
  float r0[9], r1[9], m0, m1, tm0 = 0.f, tm1 = 0.f;
  for (int mode = 0;; ++mode) {
    f32x16 o0, o1;
    float m = mode == 1 ? tm0 : tm1;
    const char* kp = wa + kvo + 4096;
    const char* wp = wo + (hq >> 1) * 256 + (w ^ 4) * 65536;
    const unsigned woff = (lane >> 4) * 1024 + (((lane & 15) ^ ((lane >> 4) << 2)) << 4);
    const u32x4 wv = {woff, woff + 3072, woff + 6144, woff + 9216};
    asm volatile("s_waitcnt lgkmcnt(0)\n\ts_nop 4\n\t" ATT_LOOP_ASM
                 : "={v[80:95]}"(o0), "={v[96:111]}"(o1), "+{v243}"(m), "+{s[40:41]}"(kp), "+{s[46:47]}"(wp) ATT_STO
                 : "{v[56:59]}"(q0), "{v[60:63]}"(q1), "{v240}"(koff), "{v241}"(voff), "{v242}"(0xDDDD), "{v[244:247]}"(wv),
                   "{s[42:43]}"(0x0000FFFF0000FFFFull), "{s45}"(mode), "{s48}"((w ^ 4) * 16384), "{s[52:53]}"(0x3F803F803F803F80ull)
                 : "scc", "memory", "s44", "s50", "s51", CLOB ATT_STC);
    if (mode != 2) {
#pragma unroll
      for (int i = 0; i < 9; ++i) r0[i] = o0[i];
      m0 = m;
    }
    if (mode != 1) {
#pragma unroll
      for (int i = 0; i < 9; ++i) r1[i] = o1[i];
      m1 = m;
    }
    if (mode == 2) break;
    if (mode == 0) {
      float big = 0.f;
#pragma unroll
      for (int i = 0; i < 8; ++i) big += fabsf(o0[i]) + fabsf(o1[i]);
      bool ok = big < 1e37f && o0[8] > 5e-20f && o0[8] < 1e30f && o1[8] > 5e-20f && o1[8] < 1e30f;
      if (allv(ok)) break;
      tm0 = tm1 = -__builtin_inff();
      const f32x16 z = {0};
#pragma unroll 1
      for (int j = 0; j < 32; ++j) {
        u32x4 kk = __builtin_amdgcn_raw_buffer_load_b128(wsr, koff, kvo + j * 2048, SC1);
        f32x16 a = mfma(kk, q0, z), c = mfma(kk, q1, z);
#pragma unroll
        for (int i = 0; i < 16; ++i) { tm0 = fmaxf(tm0, a[i]); tm1 = fmaxf(tm1, c[i]); }
      }
      tm0 = maxh(tm0);
      tm1 = maxh(tm1);
    }
  }
  ST(8);

  {
  int lane = threadIdx.x & 63;
  asm volatile("" : "+v"(lane));
  const int r = lane & 31, h = lane >> 5;
  const int q4 = (lane >> 2) & 3, pp = lane & 3, g1 = (lane >> 4) & 1;
  const int tt = 2 * qb + (hq & 1);
  if (w >= 4) {
#pragma unroll
    for (int i = 0; i < 9; ++i) { float f = r0[i]; r0[i] = r1[i]; r1[i] = f; }
    float f = m0; m0 = m1; m1 = f;
  }
  {
    if (w >= 4) lwait(FL_CONV + pp * 4);
    unsigned ea = (w < 4 ? L_EX : L_PR) + (w & 3) * 2560;
    *lp<f32x4>(ea + lane * 16) = f32x4{r1[0], r1[1], r1[2], r1[3]};
    *lp<f32x4>(ea + 1024 + lane * 16) = f32x4{r1[4], r1[5], r1[6], r1[7]};
    *lp<f32x2>(ea + 2048 + lane * 8) = f32x2{r1[8], m1};
  }
  VMBAR(0);
  ST(9);
  {
    unsigned ea = (w < 4 ? L_PR : L_EX) + (w & 3) * 2560;
    f32x4 a0 = *lp<f32x4>(ea + lane * 16), a1 = *lp<f32x4>(ea + 1024 + lane * 16);
    f32x2 lm = *lp<f32x2>(ea + 2048 + lane * 8);
    float M = fmaxf(m0, lm[1]);
    float fa = __builtin_amdgcn_exp2f(m0 - M), fb = __builtin_amdgcn_exp2f(lm[1] - M);
    float inv = __builtin_amdgcn_rcpf(fa * r0[8] + fb * lm[0]);
    fa *= inv; fb *= inv;
    u32x4 o = {pk(fa * r0[0] + fb * a0[0], fa * r0[1] + fb * a0[1]), pk(fa * r0[2] + fb * a0[2], fa * r0[3] + fb * a0[3]),
               pk(fa * r0[4] + fb * a1[0], fa * r0[5] + fb * a1[1]), pk(fa * r0[6] + fb * a1[2], fa * r0[7] + fb * a1[3])};
#pragma unroll
    for (int i = 0; i < 4; ++i) o[i] = min(o[i], 0xFFFFFFFEu);
    __builtin_amdgcn_raw_buffer_store_b128(o, wsr, WS_O + ((2 * qb + (w >> 2)) * 32 + head) * 1024 + lane * 16, 0, SC1);
  }
  u32x4 us = *(const u32x4*)(u + (qb * 64 + lane) * 1024 + hq * 128 + w * 16);
  u32x4 ds = *(const u32x4*)(Dsk + hq * 128 + w * 16);
  u32x4 wf[16];
  {
    const lds_t a = lb((w >> 2) * 65536 + (4 * h + q4) * 256 + ((64 * (w & 3) + 32 * g1 + 8 * (((pp & 1) << 1) | (pp >> 1))) ^ (64 * q4)));
#pragma unroll
    for (int i = 0; i < 16; ++i) wf[i] = trd2(a + i * 4096, a + i * 4096 + 2048);
  }
  poll(wa + WS_F2 + (lane < 4 ? hq * 32 + 4 * w + lane : w * 32 + qb) * 128, tag);
  u32x4 yv = __builtin_amdgcn_raw_buffer_load_b128(wsr, WS_YT + (hq * 64 + 8 * w + (lane >> 3)) * 4096 + qb * 128 + (lane & 7) * 16, 0, SC1);
  BAR();
  ST(10);
  {
    unsigned yl = w * 1024;
    *lp<u32x4>(yl + lane * 16) = yv;
    lds_t a = lb(yl + q4 * 128 + (lane >> 4) * 32 + pp * 8);
    u32x4 y = trd2(a, a + 512), o;
#pragma unroll
    for (int i = 0; i < 4; ++i) {
      unsigned sk = pk(b2f(us[i] & 0xFFFF) * b2f(ds[i] & 0xFFFF), b2f(us[i] >> 16) * b2f(ds[i] >> 16));
      o[i] = pk(b2f(y[i] & 0xFFFF) + b2f(sk & 0xFFFF), b2f(y[i] >> 16) + b2f(sk >> 16));
    }
    *(u32x4*)(out + (qb * 64 + lane) * 2048 + hq * 128 + w * 16) = o;
  }
  ST(11);
  {
    u32x4 v[4];
    const unsigned vo = WS_O + (tt * 32 + 4 * w) * 1024 + lane * 16;
    const lds_t ol = lb(L_S + 4 * w * 1024 + lane * 16);
  #pragma unroll 1
  for (unsigned s = 0; s < SPIN_CAP; ++s) {
      CFENCE();
      unsigned mx = 0;
#pragma unroll
      for (int i = 0; i < 4; ++i) v[i] = __builtin_amdgcn_raw_buffer_load_b128(wsr, vo + i * 1024, 0, SC1);
#pragma unroll
      for (int i = 0; i < 4; ++i) mx = max(max(mx, max(v[i][0], v[i][1])), max(v[i][2], v[i][3]));
      if (allv(mx != 0xFFFFFFFFu)) break;
    }
#pragma unroll
    for (int i = 0; i < 4; ++i) *lp<u32x4>(ol + i * 1024) = v[i];
  }
  BAR();
  ST(12);
  if (b == 0 && threadIdx.x == 0) __hip_atomic_store((AS1 unsigned*)ws, epoch, __ATOMIC_RELAXED, __HIP_MEMORY_SCOPE_AGENT);
  {
    f32x16 acc = {0};
    const lds_t ob = lb(L_S + (w >> 2) * 16384 + lane * 16);
#pragma unroll
    for (int i = 0; i < 16; ++i) acc = mfma(wf[i], *lp<u32x4>(ob + i * 1024), acc);
    lds_t ea = lb(16384 + w * 2048 + lane * 16);
    f32x4 k0, k1;
    if (w < 4) {
      *lp<f32x4>(ea) = f32x4{acc[8], acc[9], acc[10], acc[11]}; *lp<f32x4>(ea + 1024) = f32x4{acc[12], acc[13], acc[14], acc[15]};
      k0 = f32x4{acc[0], acc[1], acc[2], acc[3]}; k1 = f32x4{acc[4], acc[5], acc[6], acc[7]};
    } else {
      *lp<f32x4>(ea) = f32x4{acc[0], acc[1], acc[2], acc[3]}; *lp<f32x4>(ea + 1024) = f32x4{acc[4], acc[5], acc[6], acc[7]};
      k0 = f32x4{acc[8], acc[9], acc[10], acc[11]}; k1 = f32x4{acc[12], acc[13], acc[14], acc[15]};
    }
    BAR();
    ea = lb(16384 + (w ^ 4) * 2048 + lane * 16);
    k0 += *lp<f32x4>(ea); k1 += *lp<f32x4>(ea + 1024);
    *(u32x4*)(out + (tt * 32 + r) * 2048 + 1024 + (hq >> 1) * 256 + (w & 3) * 64 + (w >> 2) * 32 + h * 16) =
        u32x4{pk(k0[0], k0[1]), pk(k0[2], k0[3]), pk(k1[0], k1[1]), pk(k1[2], k1[3])};
  }
  ST(13);
  }
}

extern "C" void kernel_launch(void* const* d_in, const int* in_sizes, int n_in, void* d_out, int out_size, void* d_ws,
                              size_t ws_size, hipStream_t stream) {
  fused<<<256, 512, 0, stream>>>((const char*)d_in[0], (const u16*)d_in[1], (const u16*)d_in[2], (const u16*)d_in[3],
                                 (const u16*)d_in[4], (const u16*)d_in[5], (const u16*)d_in[6], (const u16*)d_in[7],
                                 (const char*)d_in[8], (const char*)d_in[9], (const char*)d_in[10], (const char*)d_in[11],
                                 (char*)d_out, (char*)d_ws);
}
```

```cpp
#include <hip/hip_runtime.h>
typedef float f32x16 __attribute__((ext_vector_type(16)));
typedef float f32x4 __attribute__((ext_vector_type(4)));
typedef float f32x2 __attribute__((ext_vector_type(2)));
typedef __bf16 bf16x8 __attribute__((ext_vector_type(8)));
typedef __bf16 bf16x2 __attribute__((ext_vector_type(2)));
typedef short s16x4 __attribute__((ext_vector_type(4)));
typedef unsigned u32x4 __attribute__((ext_vector_type(4)));
typedef unsigned u32x2 __attribute__((ext_vector_type(2)));
typedef unsigned short u16;
typedef unsigned long long u64;
#define AS3 __attribute__((address_space(3)))
#define AS1 __attribute__((address_space(1)))
#define DEV __device__ __forceinline__

#define ATT_INI_ASM \
  "v_mov_b64 v[160:161], 0\n\t" \
  "v_mov_b64 v[162:163], 0\n\t" \
  "v_mov_b64 v[172:173], 0\n\t" \
  "s_mov_b32 exec_lo, 0x110000\n\t" \
  "s_mov_b32 exec_hi, 0x110000\n\t" \
  "v_mov_b64 v[172:173], s[52:53]\n\t" \
  "s_not_b64 exec, s[42:43]\n\t" \
  "v_mfma_f32_32x32x16_bf16 v[64:79], v[160:163], v[160:163], 0\n\t" \
  "v_mov_b64 v[164:165], v[172:173]\n\t" \
  "v_mov_b64 v[166:167], v[172:173]\n\t" \
  "v_mov_b64 v[168:169], v[172:173]\n\t" \
  "v_mov_b64 v[170:171], v[172:173]\n\t" \
  "v_mfma_f32_32x32x16_bf16 v[80:95], v[160:163], v[160:163], 0\n\t" \
  "v_mov_b64 v[176:177], v[172:173]\n\t" \
  "v_mov_b64 v[178:179], v[172:173]\n\t" \
  "v_mov_b64 v[180:181], v[172:173]\n\t" \
  "v_mov_b64 v[182:183], v[172:173]\n\t" \
  "v_mfma_f32_32x32x16_bf16 v[96:111], v[160:163], v[160:163], 0\n\t" \
  "v_mov_b64 v[188:189], v[172:173]\n\t" \
  "v_mov_b64 v[190:191], v[172:173]\n\t" \
  "v_mov_b64 v[192:193], v[172:173]\n\t" \
  "v_mov_b64 v[194:195], v[172:173]\n\t" \
  "v_mfma_f32_32x32x16_bf16 v[112:127], v[160:163], v[160:163], 0\n\t" \
  "v_mov_b64 v[200:201], v[172:173]\n\t" \
  "v_mov_b64 v[202:203], v[172:173]\n\t" \
  "v_mov_b64 v[204:205], v[172:173]\n\t" \
  "v_mov_b64 v[206:207], v[172:173]\n\t" \
  "s_mov_b64 exec, -1\n\t"
#define ATT_PRE_ASM \
  "global_load_dwordx4 v[160:163], v240, s[40:41] offset:-4096 sc1\n\t" \
  "s_mov_b64 exec, s[42:43]\n\t" \
  "global_load_dwordx4 v[164:167], v240, s[40:41] offset:-3072 sc1\n\t" \
  "global_load_dwordx4 v[168:171], v240, s[40:41] offset:-2816 sc1\n\t" \
  "s_mov_b64 exec, -1\n\t" \
  "global_load_dwordx4 v[172:175], v240, s[40:41] offset:-2048 sc1\n\t" \
  "s_mov_b64 exec, s[42:43]\n\t" \
  "global_load_dwordx4 v[176:179], v240, s[40:41] offset:-1024 sc1\n\t" \
  "global_load_dwordx4 v[180:183], v240, s[40:41] offset:-768 sc1\n\t" \
  "s_mov_b64 exec, -1\n\t" \
  "global_load_dwordx4 v[184:187], v240, s[40:41] offset:0 sc1\n\t" \
  "s_mov_b64 exec, s[42:43]\n\t" \
  "global_load_dwordx4 v[188:191], v240, s[40:41] offset:1024 sc1\n\t" \
  "global_load_dwordx4 v[192:195], v240, s[40:41] offset:1280 sc1\n\t" \
  "s_mov_b64 exec, -1\n\t" \
  "global_load_dwordx4 v[196:199], v240, s[40:41] offset:2048 sc1\n\t" \
  "s_mov_b64 exec, s[42:43]\n\t" \
  "global_load_dwordx4 v[200:203], v240, s[40:41] offset:3072 sc1\n\t" \
  "global_load_dwordx4 v[204:207], v240, s[40:41] offset:3328 sc1\n\t" \
  "s_mov_b64 exec, -1\n\t"
#define ATT_LOOP_ASM \
  "s_mov_b32 m0, s48\n\t" \
  "s_mov_b32 s44, 2\n\t" \
  "s_waitcnt vmcnt(11)\n\t" \
  "v_mfma_f32_32x32x16_bf16 v[128:143], v[160:163], v[48:51], 0\n\t" \
  "s_nop 7\n\t" \
  "s_nop 7\n\t" \
  "v_exp_f32 v128, v128\n\t" \
  "v_exp_f32 v129, v129\n\t" \
  "s_waitcnt vmcnt(9)\n\t" \
  "1:\n\t" \
  ATT_ST \
  "v_exp_f32 v130, v130\n\t" \
  "v_exp_f32 v131, v131\n\t" \
  "v_exp_f32 v132, v132\n\t" \
  "v_mfma_f32_32x32x16_bf16 v[144:159], v[160:163], v[52:55], 0\n\t" \
  "v_exp_f32 v133, v133\n\t" \
  "v_exp_f32 v134, v134\n\t" \
  "v_exp_f32 v135, v135\n\t" \
  "v_exp_f32 v136, v136\n\t" \
  "v_exp_f32 v137, v137\n\t" \
  "v_exp_f32 v138, v138\n\t" \
  "v_smfmac_f32_32x32x32_bf16 v[64:79], v[164:167], v[128:135], v242\n\t" \
  "global_load_lds_dwordx4 v244, s[46:47] offset:0\n\t" \
  "v_exp_f32 v139, v139\n\t" \
  "v_exp_f32 v140, v140\n\t" \
  "v_exp_f32 v141, v141\n\t" \
  "v_exp_f32 v142, v142\n\t" \
  "v_exp_f32 v143, v143\n\t" \
  "v_exp_f32 v144, v144\n\t" \
  "v_exp_f32 v145, v145\n\t" \
  "v_smfmac_f32_32x32x32_bf16 v[64:79], v[168:171], v[136:143], v242\n\t" \
  "v_exp_f32 v146, v146\n\t" \
  "v_exp_f32 v147, v147\n\t" \
  "v_exp_f32 v148, v148\n\t" \
  "v_mfma_f32_32x32x16_bf16 v[128:143], v[160:163], v[56:59], 0\n\t" \
  "v_exp_f32 v149, v149\n\t" \
  "v_exp_f32 v150, v150\n\t" \
  "v_exp_f32 v151, v151\n\t" \
  "v_exp_f32 v152, v152\n\t" \
  "v_exp_f32 v153, v153\n\t" \
  "v_exp_f32 v154, v154\n\t" \
  "v_smfmac_f32_32x32x32_bf16 v[80:95], v[164:167], v[144:151], v242\n\t" \
  "s_add_u32 s40, s40, 8192\n\t" \
  "s_addc_u32 s41, s41, 0\n\t" \
  "v_exp_f32 v155, v155\n\t" \
  "v_exp_f32 v156, v156\n\t" \
  "v_exp_f32 v157, v157\n\t" \
  "v_exp_f32 v158, v158\n\t" \
  "v_exp_f32 v159, v159\n\t" \
  "v_exp_f32 v128, v128\n\t" \
  "v_exp_f32 v129, v129\n\t" \
  "v_smfmac_f32_32x32x32_bf16 v[80:95], v[168:171], v[152:159], v242\n\t" \
  "v_exp_f32 v130, v130\n\t" \
  "v_exp_f32 v131, v131\n\t" \
  "v_exp_f32 v132, v132\n\t" \
  "v_mfma_f32_32x32x16_bf16 v[144:159], v[160:163], v[60:63], 0\n\t" \
  "v_exp_f32 v133, v133\n\t" \
  "v_exp_f32 v134, v134\n\t" \
  "v_exp_f32 v135, v135\n\t" \
  "v_exp_f32 v136, v136\n\t" \
  "v_exp_f32 v137, v137\n\t" \
  "v_exp_f32 v138, v138\n\t" \
  "v_smfmac_f32_32x32x32_bf16 v[96:111], v[164:167], v[128:135], v242\n\t" \
  "global_load_dwordx4 v[160:163], v240, s[40:41] offset:-4096 sc1\n\t" \
  "v_exp_f32 v139, v139\n\t" \
  "v_exp_f32 v140, v140\n\t" \
  "v_exp_f32 v141, v141\n\t" \
  "v_exp_f32 v142, v142\n\t" \
  "v_exp_f32 v143, v143\n\t" \
  "v_exp_f32 v144, v144\n\t" \
  "v_exp_f32 v145, v145\n\t" \
  "v_smfmac_f32_32x32x32_bf16 v[96:111], v[168:171], v[136:143], v242\n\t" \
  "s_waitcnt vmcnt(8)\n\t" \
  "v_exp_f32 v146, v146\n\t" \
  "v_exp_f32 v147, v147\n\t" \
  "v_exp_f32 v148, v148\n\t" \
  "v_mfma_f32_32x32x16_bf16 v[128:143], v[172:175], v[48:51], 0\n\t" \
  "v_exp_f32 v149, v149\n\t" \
  "v_exp_f32 v150, v150\n\t" \
  "v_exp_f32 v151, v151\n\t" \
  "v_exp_f32 v152, v152\n\t" \
  "v_exp_f32 v153, v153\n\t" \
  "v_exp_f32 v154, v154\n\t" \
  "v_smfmac_f32_32x32x32_bf16 v[112:127], v[164:167], v[144:151], v242\n\t" \
  "v_exp_f32 v155, v155\n\t" \
  "v_exp_f32 v156, v156\n\t" \
  "v_exp_f32 v157, v157\n\t" \
  "v_exp_f32 v158, v158\n\t" \
  "v_exp_f32 v159, v159\n\t" \
  "v_exp_f32 v128, v128\n\t" \
  "v_exp_f32 v129, v129\n\t" \
  "v_smfmac_f32_32x32x32_bf16 v[112:127], v[168:171], v[152:159], v242\n\t" \
  "s_mov_b64 exec, s[42:43]\n\t" \
  "global_load_dwordx4 v[164:167], v240, s[40:41] offset:-3072 sc1\n\t" \
  "global_load_dwordx4 v[168:171], v240, s[40:41] offset:-2816 sc1\n\t" \
  "s_mov_b64 exec, -1\n\t" \
  "v_exp_f32 v130, v130\n\t" \
  "v_exp_f32 v131, v131\n\t" \
  "v_exp_f32 v132, v132\n\t" \
  "v_mfma_f32_32x32x16_bf16 v[144:159], v[172:175], v[52:55], 0\n\t" \
  "v_exp_f32 v133, v133\n\t" \
  "v_exp_f32 v134, v134\n\t" \
  "v_exp_f32 v135, v135\n\t" \
  "v_exp_f32 v136, v136\n\t" \
  "v_exp_f32 v137, v137\n\t" \
  "v_exp_f32 v138, v138\n\t" \
  "v_smfmac_f32_32x32x32_bf16 v[64:79], v[176:179], v[128:135], v242\n\t" \
  "global_load_lds_dwordx4 v245, s[46:47] offset:1024\n\t" \
  "v_exp_f32 v139, v139\n\t" \
  "v_exp_f32 v140, v140\n\t" \
  "v_exp_f32 v141, v141\n\t" \
  "v_exp_f32 v142, v142\n\t" \
  "v_exp_f32 v143, v143\n\t" \
  "v_exp_f32 v144, v144\n\t" \
  "v_exp_f32 v145, v145\n\t" \
  "v_smfmac_f32_32x32x32_bf16 v[64:79], v[180:183], v[136:143], v242\n\t" \
  "v_exp_f32 v146, v146\n\t" \
  "v_exp_f32 v147, v147\n\t" \
  "v_exp_f32 v148, v148\n\t" \
  "v_mfma_f32_32x32x16_bf16 v[128:143], v[172:175], v[56:59], 0\n\t" \
  "v_exp_f32 v149, v149\n\t" \
  "v_exp_f32 v150, v150\n\t" \
  "v_exp_f32 v151, v151\n\t" \
  "v_exp_f32 v152, v152\n\t" \
  "v_exp_f32 v153, v153\n\t" \
  "v_exp_f32 v154, v154\n\t" \
  "v_smfmac_f32_32x32x32_bf16 v[80:95], v[176:179], v[144:151], v242\n\t" \
  "v_exp_f32 v155, v155\n\t" \
  "v_exp_f32 v156, v156\n\t" \
  "v_exp_f32 v157, v157\n\t" \
  "v_exp_f32 v158, v158\n\t" \
  "v_exp_f32 v159, v159\n\t" \
  "v_exp_f32 v128, v128\n\t" \
  "v_exp_f32 v129, v129\n\t" \
  "v_smfmac_f32_32x32x32_bf16 v[80:95], v[180:183], v[152:159], v242\n\t" \
  "v_exp_f32 v130, v130\n\t" \
  "v_exp_f32 v131, v131\n\t" \
  "v_exp_f32 v132, v132\n\t" \
  "v_mfma_f32_32x32x16_bf16 v[144:159], v[172:175], v[60:63], 0\n\t" \
  "v_exp_f32 v133, v133\n\t" \
  "v_exp_f32 v134, v134\n\t" \
  "v_exp_f32 v135, v135\n\t" \
  "v_exp_f32 v136, v136\n\t" \
  "v_exp_f32 v137, v137\n\t" \
  "v_exp_f32 v138, v138\n\t" \
  "v_smfmac_f32_32x32x32_bf16 v[96:111], v[176:179], v[128:135], v242\n\t" \
  "global_load_dwordx4 v[172:175], v240, s[40:41] offset:-2048 sc1\n\t" \
  "v_exp_f32 v139, v139\n\t" \
  "v_exp_f32 v140, v140\n\t" \
  "v_exp_f32 v141, v141\n\t" \
  "v_exp_f32 v142, v142\n\t" \
  "v_exp_f32 v143, v143\n\t" \
  "v_exp_f32 v144, v144\n\t" \
  "v_exp_f32 v145, v145\n\t" \
  "v_smfmac_f32_32x32x32_bf16 v[96:111], v[180:183], v[136:143], v242\n\t" \
  "s_waitcnt vmcnt(9)\n\t" \
  "v_exp_f32 v146, v146\n\t" \
  "v_exp_f32 v147, v147\n\t" \
  "v_exp_f32 v148, v148\n\t" \
  "v_mfma_f32_32x32x16_bf16 v[128:143], v[184:187], v[48:51], 0\n\t" \
  "v_exp_f32 v149, v149\n\t" \
  "v_exp_f32 v150, v150\n\t" \
  "v_exp_f32 v151, v151\n\t" \
  "v_exp_f32 v152, v152\n\t" \
  "v_exp_f32 v153, v153\n\t" \
  "v_exp_f32 v154, v154\n\t" \
  "v_smfmac_f32_32x32x32_bf16 v[112:127], v[176:179], v[144:151], v242\n\t" \
  "v_exp_f32 v155, v155\n\t" \
  "v_exp_f32 v156, v156\n\t" \
  "v_exp_f32 v157, v157\n\t" \
  "v_exp_f32 v158, v158\n\t" \
  "v_exp_f32 v159, v159\n\t" \
  "v_exp_f32 v128, v128\n\t" \
  "v_exp_f32 v129, v129\n\t" \
  "v_smfmac_f32_32x32x32_bf16 v[112:127], v[180:183], v[152:159], v242\n\t" \
  "s_mov_b64 exec, s[42:43]\n\t" \
  "global_load_dwordx4 v[176:179], v240, s[40:41] offset:-1024 sc1\n\t" \
  "global_load_dwordx4 v[180:183], v240, s[40:41] offset:-768 sc1\n\t" \
  "s_mov_b64 exec, -1\n\t" \
  "v_exp_f32 v130, v130\n\t" \
  "v_exp_f32 v131, v131\n\t" \
  "v_exp_f32 v132, v132\n\t" \
  "v_mfma_f32_32x32x16_bf16 v[144:159], v[184:187], v[52:55], 0\n\t" \
  "v_exp_f32 v133, v133\n\t" \
  "v_exp_f32 v134, v134\n\t" \
  "v_exp_f32 v135, v135\n\t" \
  "v_exp_f32 v136, v136\n\t" \
  "v_exp_f32 v137, v137\n\t" \
  "v_exp_f32 v138, v138\n\t" \
  "v_smfmac_f32_32x32x32_bf16 v[64:79], v[188:191], v[128:135], v242\n\t" \
  "global_load_lds_dwordx4 v246, s[46:47] offset:2048\n\t" \
  "v_exp_f32 v139, v139\n\t" \
  "v_exp_f32 v140, v140\n\t" \
  "v_exp_f32 v141, v141\n\t" \
  "v_exp_f32 v142, v142\n\t" \
  "v_exp_f32 v143, v143\n\t" \
  "v_exp_f32 v144, v144\n\t" \
  "v_exp_f32 v145, v145\n\t" \
  "v_smfmac_f32_32x32x32_bf16 v[64:79], v[192:195], v[136:143], v242\n\t" \
  "v_exp_f32 v146, v146\n\t" \
  "v_exp_f32 v147, v147\n\t" \
  "v_exp_f32 v148, v148\n\t" \
  "v_mfma_f32_32x32x16_bf16 v[128:143], v[184:187], v[56:59], 0\n\t" \
  "v_exp_f32 v149, v149\n\t" \
  "v_exp_f32 v150, v150\n\t" \
  "v_exp_f32 v151, v151\n\t" \
  "v_exp_f32 v152, v152\n\t" \
  "v_exp_f32 v153, v153\n\t" \
  "v_exp_f32 v154, v154\n\t" \
  "v_smfmac_f32_32x32x32_bf16 v[80:95], v[188:191], v[144:151], v242\n\t" \
  "v_exp_f32 v155, v155\n\t" \
  "v_exp_f32 v156, v156\n\t" \
  "v_exp_f32 v157, v157\n\t" \
  "v_exp_f32 v158, v158\n\t" \
  "v_exp_f32 v159, v159\n\t" \
  "v_exp_f32 v128, v128\n\t" \
  "v_exp_f32 v129, v129\n\t" \
  "v_smfmac_f32_32x32x32_bf16 v[80:95], v[192:195], v[152:159], v242\n\t" \
  "v_exp_f32 v130, v130\n\t" \
  "v_exp_f32 v131, v131\n\t" \
  "v_exp_f32 v132, v132\n\t" \
  "v_mfma_f32_32x32x16_bf16 v[144:159], v[184:187], v[60:63], 0\n\t" \
  "v_exp_f32 v133, v133\n\t" \
  "v_exp_f32 v134, v134\n\t" \
  "v_exp_f32 v135, v135\n\t" \
  "v_exp_f32 v136, v136\n\t" \
  "v_exp_f32 v137, v137\n\t" \
  "v_exp_f32 v138, v138\n\t" \
  "v_smfmac_f32_32x32x32_bf16 v[96:111], v[188:191], v[128:135], v242\n\t" \
  "global_load_dwordx4 v[184:187], v240, s[40:41] offset:0 sc1\n\t" \
  "v_exp_f32 v139, v139\n\t" \
  "v_exp_f32 v140, v140\n\t" \
  "v_exp_f32 v141, v141\n\t" \
  "v_exp_f32 v142, v142\n\t" \
  "v_exp_f32 v143, v143\n\t" \
  "v_exp_f32 v144, v144\n\t" \
  "v_exp_f32 v145, v145\n\t" \
  "v_smfmac_f32_32x32x32_bf16 v[96:111], v[192:195], v[136:143], v242\n\t" \
  "s_waitcnt vmcnt(10)\n\t" \
  "v_exp_f32 v146, v146\n\t" \
  "v_exp_f32 v147, v147\n\t" \
  "v_exp_f32 v148, v148\n\t" \
  "v_mfma_f32_32x32x16_bf16 v[128:143], v[196:199], v[48:51], 0\n\t" \
  "v_exp_f32 v149, v149\n\t" \
  "v_exp_f32 v150, v150\n\t" \
  "v_exp_f32 v151, v151\n\t" \
  "v_exp_f32 v152, v152\n\t" \
  "v_exp_f32 v153, v153\n\t" \
  "v_exp_f32 v154, v154\n\t" \
  "v_smfmac_f32_32x32x32_bf16 v[112:127], v[188:191], v[144:151], v242\n\t" \
  "v_exp_f32 v155, v155\n\t" \
  "v_exp_f32 v156, v156\n\t" \
  "v_exp_f32 v157, v157\n\t" \
  "v_exp_f32 v158, v158\n\t" \
  "v_exp_f32 v159, v159\n\t" \
  "v_exp_f32 v128, v128\n\t" \
  "v_exp_f32 v129, v129\n\t" \
  "v_smfmac_f32_32x32x32_bf16 v[112:127], v[192:195], v[152:159], v242\n\t" \
  "s_mov_b64 exec, s[42:43]\n\t" \
  "global_load_dwordx4 v[188:191], v240, s[40:41] offset:1024 sc1\n\t" \
  "global_load_dwordx4 v[192:195], v240, s[40:41] offset:1280 sc1\n\t" \
  "s_mov_b64 exec, -1\n\t" \
  "v_exp_f32 v130, v130\n\t" \
  "v_exp_f32 v131, v131\n\t" \
  "v_exp_f32 v132, v132\n\t" \
  "v_mfma_f32_32x32x16_bf16 v[144:159], v[196:199], v[52:55], 0\n\t" \
  "v_exp_f32 v133, v133\n\t" \
  "v_exp_f32 v134, v134\n\t" \
  "v_exp_f32 v135, v135\n\t" \
  "v_exp_f32 v136, v136\n\t" \
  "v_exp_f32 v137, v137\n\t" \
  "v_exp_f32 v138, v138\n\t" \
  "v_smfmac_f32_32x32x32_bf16 v[64:79], v[200:203], v[128:135], v242\n\t" \
  "global_load_lds_dwordx4 v247, s[46:47] offset:3072\n\t" \
  "v_exp_f32 v139, v139\n\t" \
  "v_exp_f32 v140, v140\n\t" \
  "v_exp_f32 v141, v141\n\t" \
  "v_exp_f32 v142, v142\n\t" \
  "v_exp_f32 v143, v143\n\t" \
  "v_exp_f32 v144, v144\n\t" \
  "v_exp_f32 v145, v145\n\t" \
  "v_smfmac_f32_32x32x32_bf16 v[64:79], v[204:207], v[136:143], v242\n\t" \
  "s_cmp_lg_u32 s44, 2\n\t" \
  "s_cbranch_scc1 2f\n\t" \
  "s_mov_b64 exec, 1\n\t" \
  "global_store_dwordx2 v208, v[212:213], s[50:51] sc1\n\t" \
  "s_mov_b64 exec, -1\n\t" \
  "2:\n\t" \
  "v_exp_f32 v146, v146\n\t" \
  "v_exp_f32 v147, v147\n\t" \
  "v_exp_f32 v148, v148\n\t" \
  "v_mfma_f32_32x32x16_bf16 v[128:143], v[196:199], v[56:59], 0\n\t" \
  "v_exp_f32 v149, v149\n\t" \
  "v_exp_f32 v150, v150\n\t" \
  "v_exp_f32 v151, v151\n\t" \
  "v_exp_f32 v152, v152\n\t" \
  "v_exp_f32 v153, v153\n\t" \
  "v_exp_f32 v154, v154\n\t" \
  "v_smfmac_f32_32x32x32_bf16 v[80:95], v[200:203], v[144:151], v242\n\t" \
  "s_add_u32 s46, s46, 16384\n\t" \
  "s_addc_u32 s47, s47, 0\n\t" \
  "v_exp_f32 v155, v155\n\t" \
  "v_exp_f32 v156, v156\n\t" \
  "v_exp_f32 v157, v157\n\t" \
  "v_exp_f32 v158, v158\n\t" \
  "v_exp_f32 v159, v159\n\t" \
  "v_exp_f32 v128, v128\n\t" \
  "v_exp_f32 v129, v129\n\t" \
  "v_smfmac_f32_32x32x32_bf16 v[80:95], v[204:207], v[152:159], v242\n\t" \
  "s_add_u32 m0, m0, 4096\n\t" \
  "v_exp_f32 v130, v130\n\t" \
  "v_exp_f32 v131, v131\n\t" \
  "v_exp_f32 v132, v132\n\t" \
  "v_mfma_f32_32x32x16_bf16 v[144:159], v[196:199], v[60:63], 0\n\t" \
  "v_exp_f32 v133, v133\n\t" \
  "v_exp_f32 v134, v134\n\t" \
  "v_exp_f32 v135, v135\n\t" \
  "v_exp_f32 v136, v136\n\t" \
  "v_exp_f32 v137, v137\n\t" \
  "v_exp_f32 v138, v138\n\t" \
  "v_smfmac_f32_32x32x32_bf16 v[96:111], v[200:203], v[128:135], v242\n\t" \
  "global_load_dwordx4 v[196:199], v240, s[40:41] offset:2048 sc1\n\t" \
  "v_exp_f32 v139, v139\n\t" \
  "v_exp_f32 v140, v140\n\t" \
  "v_exp_f32 v141, v141\n\t" \
  "v_exp_f32 v142, v142\n\t" \
  "v_exp_f32 v143, v143\n\t" \
  "v_exp_f32 v144, v144\n\t" \
  "v_exp_f32 v145, v145\n\t" \
  "v_smfmac_f32_32x32x32_bf16 v[96:111], v[204:207], v[136:143], v242\n\t" \
  "s_waitcnt vmcnt(10)\n\t" \
  "v_exp_f32 v146, v146\n\t" \
  "v_exp_f32 v147, v147\n\t" \
  "v_exp_f32 v148, v148\n\t" \
  "v_mfma_f32_32x32x16_bf16 v[128:143], v[160:163], v[48:51], 0\n\t" \
  "v_exp_f32 v149, v149\n\t" \
  "v_exp_f32 v150, v150\n\t" \
  "v_exp_f32 v151, v151\n\t" \
  "v_exp_f32 v152, v152\n\t" \
  "v_exp_f32 v153, v153\n\t" \
  "v_exp_f32 v154, v154\n\t" \
  "v_smfmac_f32_32x32x32_bf16 v[112:127], v[200:203], v[144:151], v242\n\t" \
  "s_sub_u32 s44, s44, 1\n\t" \
  "v_exp_f32 v155, v155\n\t" \
  "v_exp_f32 v156, v156\n\t" \
  "v_exp_f32 v157, v157\n\t" \
  "v_exp_f32 v158, v158\n\t" \
  "v_exp_f32 v159, v159\n\t" \
  "v_exp_f32 v128, v128\n\t" \
  "v_exp_f32 v129, v129\n\t" \
  "v_smfmac_f32_32x32x32_bf16 v[112:127], v[204:207], v[152:159], v242\n\t" \
  "s_mov_b64 exec, s[42:43]\n\t" \
  "global_load_dwordx4 v[200:203], v240, s[40:41] offset:3072 sc1\n\t" \
  "global_load_dwordx4 v[204:207], v240, s[40:41] offset:3328 sc1\n\t" \
  "s_mov_b64 exec, -1\n\t" \
  "s_cbranch_scc0 1b\n\t" \
  ATT_ST \
  "v_exp_f32 v130, v130\n\t" \
  "v_exp_f32 v131, v131\n\t" \
  "v_exp_f32 v132, v132\n\t" \
  "v_mfma_f32_32x32x16_bf16 v[144:159], v[160:163], v[52:55], 0\n\t" \
  "v_exp_f32 v133, v133\n\t" \
  "v_exp_f32 v134, v134\n\t" \
  "v_exp_f32 v135, v135\n\t" \
  "v_exp_f32 v136, v136\n\t" \
  "v_exp_f32 v137, v137\n\t" \
  "v_exp_f32 v138, v138\n\t" \
  "v_smfmac_f32_32x32x32_bf16 v[64:79], v[164:167], v[128:135], v242\n\t" \
  "global_load_lds_dwordx4 v244, s[46:47] offset:0\n\t" \
  "global_load_lds_dwordx4 v245, s[46:47] offset:1024\n\t" \
  "v_exp_f32 v139, v139\n\t" \
  "v_exp_f32 v140, v140\n\t" \
  "v_exp_f32 v141, v141\n\t" \
  "v_exp_f32 v142, v142\n\t" \
  "v_exp_f32 v143, v143\n\t" \
  "v_exp_f32 v144, v144\n\t" \
  "v_exp_f32 v145, v145\n\t" \
  "v_smfmac_f32_32x32x32_bf16 v[64:79], v[168:171], v[136:143], v242\n\t" \
  "v_exp_f32 v146, v146\n\t" \
  "v_exp_f32 v147, v147\n\t" \
  "v_exp_f32 v148, v148\n\t" \
  "v_mfma_f32_32x32x16_bf16 v[128:143], v[160:163], v[56:59], 0\n\t" \
  "v_exp_f32 v149, v149\n\t" \
  "v_exp_f32 v150, v150\n\t" \
  "v_exp_f32 v151, v151\n\t" \
  "v_exp_f32 v152, v152\n\t" \
  "v_exp_f32 v153, v153\n\t" \
  "v_exp_f32 v154, v154\n\t" \
  "v_smfmac_f32_32x32x32_bf16 v[80:95], v[164:167], v[144:151], v242\n\t" \
  "global_load_lds_dwordx4 v246, s[46:47] offset:2048\n\t" \
  "global_load_lds_dwordx4 v247, s[46:47] offset:3072\n\t" \
  "v_exp_f32 v155, v155\n\t" \
  "v_exp_f32 v156, v156\n\t" \
  "v_exp_f32 v157, v157\n\t" \
  "v_exp_f32 v158, v158\n\t" \
  "v_exp_f32 v159, v159\n\t" \
  "v_exp_f32 v128, v128\n\t" \
  "v_exp_f32 v129, v129\n\t" \
  "v_smfmac_f32_32x32x32_bf16 v[80:95], v[168:171], v[152:159], v242\n\t" \
  "v_exp_f32 v130, v130\n\t" \
  "v_exp_f32 v131, v131\n\t" \
  "v_exp_f32 v132, v132\n\t" \
  "v_mfma_f32_32x32x16_bf16 v[144:159], v[160:163], v[60:63], 0\n\t" \
  "v_exp_f32 v133, v133\n\t" \
  "v_exp_f32 v134, v134\n\t" \
  "v_exp_f32 v135, v135\n\t" \
  "v_exp_f32 v136, v136\n\t" \
  "v_exp_f32 v137, v137\n\t" \
  "v_exp_f32 v138, v138\n\t" \
  "v_smfmac_f32_32x32x32_bf16 v[96:111], v[164:167], v[128:135], v242\n\t" \
  "global_load_dwordx2 v[210:211], v210, s[50:51] sc1\n\t" \
  "v_exp_f32 v139, v139\n\t" \
  "v_exp_f32 v140, v140\n\t" \
  "v_exp_f32 v141, v141\n\t" \
  "v_exp_f32 v142, v142\n\t" \
  "v_exp_f32 v143, v143\n\t" \
  "v_exp_f32 v144, v144\n\t" \
  "v_exp_f32 v145, v145\n\t" \
  "v_smfmac_f32_32x32x32_bf16 v[96:111], v[168:171], v[136:143], v242\n\t" \
  "s_waitcnt vmcnt(13)\n\t" \
  "v_exp_f32 v146, v146\n\t" \
  "v_exp_f32 v147, v147\n\t" \
  "v_exp_f32 v148, v148\n\t" \
  "v_mfma_f32_32x32x16_bf16 v[128:143], v[172:175], v[56:59], 0\n\t" \
  "v_exp_f32 v149, v149\n\t" \
  "v_exp_f32 v150, v150\n\t" \
  "v_exp_f32 v151, v151\n\t" \
  "v_exp_f32 v152, v152\n\t" \
  "v_exp_f32 v153, v153\n\t" \
  "v_exp_f32 v154, v154\n\t" \
  "v_smfmac_f32_32x32x32_bf16 v[112:127], v[164:167], v[144:151], v242\n\t" \
  "v_exp_f32 v155, v155\n\t" \
  "v_exp_f32 v156, v156\n\t" \
  "v_exp_f32 v157, v157\n\t" \
  "v_exp_f32 v158, v158\n\t" \
  "v_exp_f32 v159, v159\n\t" \
  "v_exp_f32 v128, v128\n\t" \
  "v_exp_f32 v129, v129\n\t" \
  "v_smfmac_f32_32x32x32_bf16 v[112:127], v[168:171], v[152:159], v242\n\t" \
  "v_exp_f32 v130, v130\n\t" \
  "v_exp_f32 v131, v131\n\t" \
  "v_exp_f32 v132, v132\n\t" \
  "v_mfma_f32_32x32x16_bf16 v[144:159], v[172:175], v[60:63], 0\n\t" \
  "v_exp_f32 v133, v133\n\t" \
  "v_exp_f32 v134, v134\n\t" \
  "v_exp_f32 v135, v135\n\t" \
  "v_exp_f32 v136, v136\n\t" \
  "v_exp_f32 v137, v137\n\t" \
  "v_exp_f32 v138, v138\n\t" \
  "v_smfmac_f32_32x32x32_bf16 v[96:111], v[176:179], v[128:135], v242\n\t" \
  "v_exp_f32 v139, v139\n\t" \
  "v_exp_f32 v140, v140\n\t" \
  "v_exp_f32 v141, v141\n\t" \
  "v_exp_f32 v142, v142\n\t" \
  "v_exp_f32 v143, v143\n\t" \
  "v_exp_f32 v144, v144\n\t" \
  "v_exp_f32 v145, v145\n\t" \
  "v_smfmac_f32_32x32x32_bf16 v[96:111], v[180:183], v[136:143], v242\n\t" \
  "s_waitcnt vmcnt(9)\n\t" \
  "v_exp_f32 v146, v146\n\t" \
  "v_exp_f32 v147, v147\n\t" \
  "v_exp_f32 v148, v148\n\t" \
  "v_mfma_f32_32x32x16_bf16 v[128:143], v[184:187], v[56:59], 0\n\t" \
  "v_exp_f32 v149, v149\n\t" \
  "v_exp_f32 v150, v150\n\t" \
  "v_exp_f32 v151, v151\n\t" \
  "v_exp_f32 v152, v152\n\t" \
  "v_exp_f32 v153, v153\n\t" \
  "v_exp_f32 v154, v154\n\t" \
  "v_smfmac_f32_32x32x32_bf16 v[112:127], v[176:179], v[144:151], v242\n\t" \
  "v_exp_f32 v155, v155\n\t" \
  "v_exp_f32 v156, v156\n\t" \
  "v_exp_f32 v157, v157\n\t" \
  "v_exp_f32 v158, v158\n\t" \
  "v_exp_f32 v159, v159\n\t" \
  "v_exp_f32 v128, v128\n\t" \
  "v_exp_f32 v129, v129\n\t" \
  "v_smfmac_f32_32x32x32_bf16 v[112:127], v[180:183], v[152:159], v242\n\t" \
  "v_exp_f32 v130, v130\n\t" \
  "v_exp_f32 v131, v131\n\t" \
  "v_exp_f32 v132, v132\n\t" \
  "v_mfma_f32_32x32x16_bf16 v[144:159], v[184:187], v[60:63], 0\n\t" \
  "v_exp_f32 v133, v133\n\t" \
  "v_exp_f32 v134, v134\n\t" \
  "v_exp_f32 v135, v135\n\t" \
  "v_exp_f32 v136, v136\n\t" \
  "v_exp_f32 v137, v137\n\t" \
  "v_exp_f32 v138, v138\n\t" \
  "v_smfmac_f32_32x32x32_bf16 v[96:111], v[188:191], v[128:135], v242\n\t" \
  "v_exp_f32 v139, v139\n\t" \
  "v_exp_f32 v140, v140\n\t" \
  "v_exp_f32 v141, v141\n\t" \
  "v_exp_f32 v142, v142\n\t" \
  "v_exp_f32 v143, v143\n\t" \
  "v_exp_f32 v144, v144\n\t" \
  "v_exp_f32 v145, v145\n\t" \
  "v_smfmac_f32_32x32x32_bf16 v[96:111], v[192:195], v[136:143], v242\n\t" \
  "s_waitcnt vmcnt(5)\n\t" \
  "v_exp_f32 v146, v146\n\t" \
  "v_exp_f32 v147, v147\n\t" \
  "v_exp_f32 v148, v148\n\t" \
  "v_mfma_f32_32x32x16_bf16 v[128:143], v[196:199], v[56:59], 0\n\t" \
  "v_exp_f32 v149, v149\n\t" \
  "v_exp_f32 v150, v150\n\t" \
  "v_exp_f32 v151, v151\n\t" \
  "v_exp_f32 v152, v152\n\t" \
  "v_exp_f32 v153, v153\n\t" \
  "v_exp_f32 v154, v154\n\t" \
  "v_smfmac_f32_32x32x32_bf16 v[112:127], v[188:191], v[144:151], v242\n\t" \
  "v_exp_f32 v155, v155\n\t" \
  "v_exp_f32 v156, v156\n\t" \
  "v_exp_f32 v157, v157\n\t" \
  "v_exp_f32 v158, v158\n\t" \
  "v_exp_f32 v159, v159\n\t" \
  "v_exp_f32 v128, v128\n\t" \
  "v_exp_f32 v129, v129\n\t" \
  "v_smfmac_f32_32x32x32_bf16 v[112:127], v[192:195], v[152:159], v242\n\t" \
  "ds_read_b32 v248, v248\n\t" \
  "v_exp_f32 v130, v130\n\t" \
  "v_exp_f32 v131, v131\n\t" \
  "v_exp_f32 v132, v132\n\t" \
  "v_mfma_f32_32x32x16_bf16 v[144:159], v[196:199], v[60:63], 0\n\t" \
  "v_exp_f32 v133, v133\n\t" \
  "v_exp_f32 v134, v134\n\t" \
  "v_exp_f32 v135, v135\n\t" \
  "v_exp_f32 v136, v136\n\t" \
  "v_exp_f32 v137, v137\n\t" \
  "v_exp_f32 v138, v138\n\t" \
  "v_smfmac_f32_32x32x32_bf16 v[96:111], v[200:203], v[128:135], v242\n\t" \
  "v_exp_f32 v139, v139\n\t" \
  "v_exp_f32 v140, v140\n\t" \
  "v_exp_f32 v141, v141\n\t" \
  "v_exp_f32 v142, v142\n\t" \
  "v_exp_f32 v143, v143\n\t" \
  "v_exp_f32 v144, v144\n\t" \
  "v_exp_f32 v145, v145\n\t" \
  "v_smfmac_f32_32x32x32_bf16 v[96:111], v[204:207], v[136:143], v242\n\t" \
  "s_waitcnt lgkmcnt(0)\n\t" \
  "v_exp_f32 v146, v146\n\t" \
  "v_exp_f32 v147, v147\n\t" \
  "v_exp_f32 v148, v148\n\t" \
  "v_mfma_f32_32x32x16_bf16 v[128:143], v[172:175], v[48:51], 0\n\t" \
  "v_exp_f32 v149, v149\n\t" \
  "v_exp_f32 v150, v150\n\t" \
  "v_cmp_eq_u32 vcc, 0, v248\n\t" \
  "v_exp_f32 v151, v151\n\t" \
  "v_exp_f32 v152, v152\n\t" \
  "v_exp_f32 v153, v153\n\t" \
  "v_exp_f32 v154, v154\n\t" \
  "v_smfmac_f32_32x32x32_bf16 v[112:127], v[200:203], v[144:151], v242\n\t" \
  "v_exp_f32 v155, v155\n\t" \
  "v_exp_f32 v156, v156\n\t" \
  "v_exp_f32 v157, v157\n\t" \
  "v_exp_f32 v158, v158\n\t" \
  "v_exp_f32 v159, v159\n\t" \
  "v_exp_f32 v128, v128\n\t" \
  "v_exp_f32 v129, v129\n\t" \
  "v_smfmac_f32_32x32x32_bf16 v[112:127], v[204:207], v[152:159], v242\n\t" \
  "s_cbranch_vccnz 3f\n\t" \
  "ds_write_b128 v241, v[96:99] offset:1024\n\t" \
  "3:\n\t" \
  "v_exp_f32 v130, v130\n\t" \
  "v_exp_f32 v131, v131\n\t" \
  "v_exp_f32 v132, v132\n\t" \
  "v_mfma_f32_32x32x16_bf16 v[144:159], v[172:175], v[52:55], 0\n\t" \
  "v_exp_f32 v133, v133\n\t" \
  "v_exp_f32 v134, v134\n\t" \
  "v_exp_f32 v135, v135\n\t" \
  "v_exp_f32 v136, v136\n\t" \
  "v_exp_f32 v137, v137\n\t" \
  "v_exp_f32 v138, v138\n\t" \
  "v_smfmac_f32_32x32x32_bf16 v[64:79], v[176:179], v[128:135], v242\n\t" \
  "s_cbranch_vccnz 4f\n\t" \
  "ds_write_b128 v241, v[100:103] offset:2048\n\t" \
  "4:\n\t" \
  "v_exp_f32 v139, v139\n\t" \
  "v_exp_f32 v140, v140\n\t" \
  "v_exp_f32 v141, v141\n\t" \
  "v_exp_f32 v142, v142\n\t" \
  "v_exp_f32 v143, v143\n\t" \
  "v_exp_f32 v144, v144\n\t" \
  "v_exp_f32 v145, v145\n\t" \
  "v_smfmac_f32_32x32x32_bf16 v[64:79], v[180:183], v[136:143], v242\n\t" \
  "s_cbranch_vccnz 5f\n\t" \
  "ds_write_b128 v241, v[112:115] offset:3072\n\t" \
  "5:\n\t" \
  "v_exp_f32 v146, v146\n\t" \
  "v_exp_f32 v147, v147\n\t" \
  "v_exp_f32 v148, v148\n\t" \
  "v_mfma_f32_32x32x16_bf16 v[128:143], v[184:187], v[48:51], 0\n\t" \
  "v_exp_f32 v149, v149\n\t" \
  "v_exp_f32 v150, v150\n\t" \
  "v_exp_f32 v151, v151\n\t" \
  "v_exp_f32 v152, v152\n\t" \
  "v_exp_f32 v153, v153\n\t" \
  "v_exp_f32 v154, v154\n\t" \
  "v_smfmac_f32_32x32x32_bf16 v[80:95], v[176:179], v[144:151], v242\n\t" \
  "s_cbranch_vccnz 6f\n\t" \
  "ds_write_b128 v241, v[116:119] offset:4096\n\t" \
  "6:\n\t" \
  "v_exp_f32 v155, v155\n\t" \
  "v_exp_f32 v156, v156\n\t" \
  "v_exp_f32 v157, v157\n\t" \
  "v_exp_f32 v158, v158\n\t" \
  "v_exp_f32 v159, v159\n\t" \
  "v_exp_f32 v128, v128\n\t" \
  "v_exp_f32 v129, v129\n\t" \
  "v_smfmac_f32_32x32x32_bf16 v[80:95], v[180:183], v[152:159], v242\n\t" \
  "s_cbranch_vccnz 7f\n\t" \
  "ds_write2_b32 v241, v104, v120 offset1:1\n\t" \
  "7:\n\t" \
  "s_cbranch_vccnz 8f\n\t" \
  "ds_write_b32 v243, v242\n\t" \
  "8:\n\t" \
  "v_exp_f32 v130, v130\n\t" \
  "v_exp_f32 v131, v131\n\t" \
  "v_exp_f32 v132, v132\n\t" \
  "v_mfma_f32_32x32x16_bf16 v[144:159], v[184:187], v[52:55], 0\n\t" \
  "s_waitcnt vmcnt(0)\n\t" \
  "v_exp_f32 v133, v133\n\t" \
  "v_exp_f32 v134, v134\n\t" \
  "v_exp_f32 v135, v135\n\t" \
  "v_exp_f32 v136, v136\n\t" \
  "v_exp_f32 v137, v137\n\t" \
  "v_exp_f32 v138, v138\n\t" \
  "v_smfmac_f32_32x32x32_bf16 v[64:79], v[188:191], v[128:135], v242\n\t" \
  "ds_write_b32 v243, v242 offset:16\n\t" \
  "v_exp_f32 v139, v139\n\t" \
  "v_exp_f32 v140, v140\n\t" \
  "v_exp_f32 v141, v141\n\t" \
  "v_exp_f32 v142, v142\n\t" \
  "v_exp_f32 v143, v143\n\t" \
  "v_exp_f32 v144, v144\n\t" \
  "v_exp_f32 v145, v145\n\t" \
  "v_smfmac_f32_32x32x32_bf16 v[64:79], v[192:195], v[136:143], v242\n\t" \
  "ds_read_b32 v209, v251 offset:64\n\t" \
  "ds_read_b64 v[176:177], v250\n\t" \
  "v_exp_f32 v146, v146\n\t" \
  "v_exp_f32 v147, v147\n\t" \
  "v_exp_f32 v148, v148\n\t" \
  "v_mfma_f32_32x32x16_bf16 v[128:143], v[196:199], v[48:51], 0\n\t" \
  "v_exp_f32 v149, v149\n\t" \
  "v_exp_f32 v150, v150\n\t" \
  "v_exp_f32 v151, v151\n\t" \
  "v_exp_f32 v152, v152\n\t" \
  "v_exp_f32 v153, v153\n\t" \
  "v_exp_f32 v154, v154\n\t" \
  "v_smfmac_f32_32x32x32_bf16 v[80:95], v[188:191], v[144:151], v242\n\t" \
  "ds_read_b128 v[160:163], v250 offset:1024\n\t" \
  "ds_read_b128 v[164:167], v250 offset:2048\n\t" \
  "v_exp_f32 v155, v155\n\t" \
  "v_exp_f32 v156, v156\n\t" \
  "v_exp_f32 v157, v157\n\t" \
  "v_exp_f32 v158, v158\n\t" \
  "v_exp_f32 v159, v159\n\t" \
  "v_exp_f32 v128, v128\n\t" \
  "v_exp_f32 v129, v129\n\t" \
  "v_smfmac_f32_32x32x32_bf16 v[80:95], v[192:195], v[152:159], v242\n\t" \
  "ds_read_b128 v[168:171], v250 offset:3072\n\t" \
  "ds_read_b128 v[172:175], v250 offset:4096\n\t" \
  "v_exp_f32 v130, v130\n\t" \
  "v_exp_f32 v131, v131\n\t" \
  "v_exp_f32 v132, v132\n\t" \
  "v_mfma_f32_32x32x16_bf16 v[144:159], v[196:199], v[52:55], 0\n\t" \
  "v_exp_f32 v133, v133\n\t" \
  "v_exp_f32 v134, v134\n\t" \
  "v_exp_f32 v135, v135\n\t" \
  "v_exp_f32 v136, v136\n\t" \
  "v_exp_f32 v137, v137\n\t" \
  "v_exp_f32 v138, v138\n\t" \
  "v_smfmac_f32_32x32x32_bf16 v[64:79], v[200:203], v[128:135], v242\n\t" \
  "v_exp_f32 v139, v139\n\t" \
  "v_exp_f32 v140, v140\n\t" \
  "v_exp_f32 v141, v141\n\t" \
  "v_exp_f32 v142, v142\n\t" \
  "v_exp_f32 v143, v143\n\t" \
  "v_exp_f32 v144, v144\n\t" \
  "v_exp_f32 v145, v145\n\t" \
  "v_smfmac_f32_32x32x32_bf16 v[64:79], v[204:207], v[136:143], v242\n\t" \
  "ds_read_b32 v208, v251\n\t" \
  "ds_read_b64 v[178:179], v249\n\t" \
  "v_exp_f32 v146, v146\n\t" \
  "v_exp_f32 v147, v147\n\t" \
  "v_exp_f32 v148, v148\n\t" \
  "ds_read_b128 v[180:183], v249 offset:1024\n\t" \
  "v_exp_f32 v149, v149\n\t" \
  "v_exp_f32 v150, v150\n\t" \
  "ds_read_b128 v[184:187], v249 offset:2048\n\t" \
  "v_exp_f32 v151, v151\n\t" \
  "v_exp_f32 v152, v152\n\t" \
  "v_exp_f32 v153, v153\n\t" \
  "v_exp_f32 v154, v154\n\t" \
  "v_smfmac_f32_32x32x32_bf16 v[80:95], v[200:203], v[144:151], v242\n\t" \
  "ds_read_b128 v[188:191], v249 offset:3072\n\t" \
  "ds_read_b128 v[192:195], v249 offset:4096\n\t" \
  "v_exp_f32 v155, v155\n\t" \
  "v_exp_f32 v156, v156\n\t" \
  "v_exp_f32 v157, v157\n\t" \
  "v_exp_f32 v158, v158\n\t" \
  "v_exp_f32 v159, v159\n\t" \
  "s_nop 1\n\t" \
  "v_smfmac_f32_32x32x32_bf16 v[80:95], v[204:207], v[152:159], v242\n\t" \
  "s_waitcnt lgkmcnt(0)\n\t" \
  "v_add_f32 v180, v180, v64\n\t" \
  "v_add_f32 v181, v181, v65\n\t" \
  "v_add_f32 v182, v182, v66\n\t" \
  "v_add_f32 v183, v183, v67\n\t" \
  "v_add_f32 v184, v184, v68\n\t" \
  "v_add_f32 v185, v185, v69\n\t" \
  "v_add_f32 v186, v186, v70\n\t" \
  "v_add_f32 v187, v187, v71\n\t" \
  "v_add_f32 v178, v178, v72\n\t" \
  "s_nop 2\n\t" \
  "v_add_f32 v188, v188, v80\n\t" \
  "v_add_f32 v189, v189, v81\n\t" \
  "v_add_f32 v190, v190, v82\n\t" \
  "v_add_f32 v191, v191, v83\n\t" \
  "v_add_f32 v192, v192, v84\n\t" \
  "v_add_f32 v193, v193, v85\n\t" \
  "v_add_f32 v194, v194, v86\n\t" \
  "v_add_f32 v195, v195, v87\n\t" \
  "v_add_f32 v179, v179, v88\n\t" \
  ATT_ST
#define CLOB "v128", "v129", "v130", "v131", "v132", "v133", "v134", "v135", "v136", "v137", "v138", "v139", "v140", "v141", "v142", "v143", "v144", "v145", "v146", "v147", "v148", "v149", "v150", "v151", "v152", "v153", "v154", "v155", "v156", "v157", "v158", "v159"

#define MAP 1
#define ROT ((qb + 2 * hq) & 6)
#define SS 2
#define KGS 9, 10, 11, 12, 13, 14, 15, 16
constexpr int KGU[9] = {KGS, 16};
#define QF1 9
#define QFL 32
#define QD 5
#define F0_B 0
#define FV_B 3
#define UC_B 5
#define EX_B 8
#define ZA_B 6
#define PRIO_X 2
#define UW_B 8
static_assert(KGU[7] == 16 && KGU[EX_B] == 16 && UW_B >= EX_B && UW_B >= UC_B, "order");
#define PV 0
#define CLN 188
#define KF 1
#define CUMS 18, 24, 30, 36, 42, 48, 54, 56
constexpr int CUM[9] = {CUMS, 56};
constexpr bool cumok() {
  for (int c = 0; c < 8; ++c)
    if (CUM[c] < 6 * c + 6 || CUM[c] > 6 * c + 18 || CUM[c + 1] - CUM[c] > 8) return false;
  return true;
}
static_assert(cumok(), "ring");
#define DE 2
#define KQX 2
#define PRIO_VT 0
#define PRIO_KA 1
#define PRIO_VR 1
#define PRIO_VA 1
#define PRIO_Q 1

constexpr unsigned WS_F0 = 0;
constexpr unsigned WS_F1 = WS_F0 + 32768;
constexpr unsigned WS_F2 = WS_F1 + 32768;
constexpr unsigned KV_HEAD = 64 * 2048;
constexpr unsigned WS_KV = 1u << 20;
constexpr unsigned WS_UT = 5u << 20;
constexpr unsigned WS_YT = 7u << 20;
constexpr unsigned WS_O = 9u << 20;
constexpr unsigned WS_AREA = 11u << 20;
constexpr unsigned WS_A0 = 1u << 20;
constexpr unsigned WS_DBG = 512u << 10;
constexpr unsigned L_SLOT = 24576;
constexpr unsigned L_VA = 98304;
constexpr unsigned L_KF = L_VA + 4 * 8448;
constexpr unsigned L_OV = L_KF + 8320;
constexpr unsigned L_FL = L_OV + 288;
constexpr unsigned L_QF = L_FL + 240;
constexpr unsigned L_EX = L_QF + 8192;
constexpr unsigned L_GK = L_QF;
constexpr unsigned L_S = 65536, L_Y = L_S + 32768, L_X = L_Y + 8192;
constexpr unsigned L_END = 163840;
static_assert(L_EX + 10240 <= L_END && L_GK + 20480 <= L_END && L_QF % 16 == 0, "lds");
constexpr unsigned FL_CONV = L_FL, FL_C1 = L_FL + 16, FL_WQ = L_FL + 32, FL_Q = L_FL + 48, FL_PART = L_FL + 64, FL_TAB = L_FL + 80, FL_GK = L_FL + 96, FL_PK = FL_GK + 16, FL_GV = L_FL + 128, FL_PW = FL_GV + 16, FL_MG = FL_GK + 64, FL_WF = L_FL + 176, FL_KD = L_FL + 192, FL_GO = L_FL + 208, FL_C2 = L_FL + 224;

DEV float b2f(unsigned x) { return __uint_as_float(x << 16); }
DEV unsigned pk(float a, float b) { f32x2 v = {a, b}; return __builtin_bit_cast(unsigned, __builtin_convertvector(v, bf16x2)); }
DEV f32x16 mfma(u32x4 a, u32x4 b, f32x16 c) {
  return __builtin_amdgcn_mfma_f32_32x32x16_bf16(__builtin_bit_cast(bf16x8, a), __builtin_bit_cast(bf16x8, b), c, 0, 0, 0);
}
typedef float f32x8 __attribute__((ext_vector_type(8)));
typedef __bf16 bf16x16 __attribute__((ext_vector_type(16)));
DEV f32x16 smf(u32x4 a, f32x8 b, f32x16 c) {
  return __builtin_amdgcn_smfmac_f32_32x32x32_bf16(__builtin_bit_cast(bf16x8, a), __builtin_bit_cast(bf16x16, b), c, 0xDDDD, 0, 0);
}
DEV bool allv(bool c) { return __builtin_amdgcn_ballot_w64(!c) == 0; }
DEV float maxh(float m) {
  auto rr = __builtin_amdgcn_permlane32_swap(__float_as_uint(m), __float_as_uint(m), false, false);
  return fmaxf(__uint_as_float(rr[0]), __uint_as_float(rr[1]));
}
typedef AS3 char* lds_t;
DEV lds_t lb(unsigned off) { asm("" : "+v"(off)); return (lds_t)(size_t)off; }
template <class T> DEV AS3 T* lp(unsigned off) { return (AS3 T*)(size_t)(off); }
template <class T> DEV AS3 T* lp(lds_t p) { return (AS3 T*)p; }
DEV u32x2 trd(lds_t p) { return __builtin_bit_cast(u32x2, __builtin_amdgcn_ds_read_tr16_b64_v4i16((AS3 s16x4*)p)); }
DEV u32x4 trd2(lds_t a, lds_t b) { u32x2 x = trd(a), y = trd(b); return u32x4{x[0], x[1], y[0], y[1]}; }
DEV void dma(const char* sbase, unsigned voff, unsigned dst) {
  asm volatile("s_mov_b32 m0, %2\n\ts_nop 0\n\tglobal_load_lds_dwordx4 %0, %1" ::"v"(voff), "s"(sbase), "s"(dst) : "memory");
}
#define OPOL 0
#define OST(off, ...) __builtin_amdgcn_raw_buffer_store_b128(__VA_ARGS__, __builtin_amdgcn_make_buffer_rsrc(out, 0, 2048 * 2048, 0x00020000), off, 0, OPOL)
#define BAR() asm volatile("s_waitcnt lgkmcnt(0)\n\ts_barrier" ::: "memory")
#define VMW(n) asm volatile("s_waitcnt vmcnt(" #n ")" ::: "memory")
#define VMBAR(n) asm volatile("s_waitcnt vmcnt(" #n ") lgkmcnt(0)\n\ts_barrier" ::: "memory")
#define CFENCE() asm volatile("" ::: "memory")
#define SC1 16
#define ST(i)
#define ATT_ST
#define ATT_STO
#define ATT_STC
constexpr unsigned SPIN_CAP = 1u << 16;

DEV void poll(const char* p, u64 tag) {
#pragma unroll 1
  for (unsigned s = 0; s < SPIN_CAP; ++s) {
    u64 v = __hip_atomic_load((const AS1 u64*)p, __ATOMIC_RELAXED, __HIP_MEMORY_SCOPE_AGENT);
    if (allv(v == tag)) break;
    __builtin_amdgcn_s_sleep(1);
  }
  CFENCE();
}

#define SBAR() __builtin_amdgcn_sched_barrier(0)
DEV void lset(unsigned off) {
  asm volatile("s_waitcnt lgkmcnt(0)" ::: "memory");
  *(volatile AS3 unsigned*)(size_t)off = 1;
}
template <int NAP = 1> DEV void lwait(unsigned off) {
#pragma unroll 1
  for (unsigned s = 0; s < SPIN_CAP; ++s) {
    if (allv(*(volatile AS3 unsigned*)(size_t)off != 0)) break;
    __builtin_amdgcn_s_sleep(NAP);
  }
  CFENCE();
}
DEV bool lcount(unsigned off, unsigned last, int lane) {
  unsigned old = 0;
  if (lane == 0) old = __hip_atomic_fetch_add(lp<unsigned>(off), 1u, __ATOMIC_RELAXED, __HIP_MEMORY_SCOPE_WORKGROUP);
  return __builtin_amdgcn_readfirstlane(old) == last;
}

DEV f32x2 cexp(float ex, float th) {
  float m = __builtin_amdgcn_exp2f(ex);
  return f32x2{m * __builtin_amdgcn_cosf(th), m * __builtin_amdgcn_sinf(th)};
}
DEV unsigned cmul(f32x2 a, float c, float s) { return pk(__builtin_fmaf(a[0], c, -(a[1] * s)), __builtin_fmaf(a[0], s, a[1] * c)); }
DEV f32x4 mfma16(u32x4 a, u32x4 b, f32x4 c) {
  return __builtin_amdgcn_mfma_f32_16x16x32_bf16(__builtin_bit_cast(bf16x8, a), __builtin_bit_cast(bf16x8, b), c, 0, 0, 0);
}

template <int SET> DEV void conv_wave(unsigned fa, unsigned ga, f32x16 (&acc)[4]) {
  constexpr int T[4] = {SET ? 6 : 7, SET ? 5 : 4, SET ? 2 : 3, SET ? 1 : 0};
  constexpr int D[4] = {0, 1, 2, 2};
  u32x4 F[8], G[8];
  {
    lds_t f = lb(fa), g = lb(ga);
#pragma unroll
    for (int a = 0; a <= T[0]; ++a) {
      G[T[0] - a] = *lp<u32x4>(g + 512 * (T[0] - a));
      if (a < 2) F[a] = trd2(f + 512 * a + 32, f + 512 * a);
      SBAR();
    }
#pragma unroll
    for (int a = 2; a <= T[0]; ++a) F[a] = trd2(f + 512 * a + 32, f + 512 * a);
  }
#pragma unroll 1
  for (int rho = 0; rho < 4; ++rho) {
    fa += 128; ga -= 128;
    lds_t f = lb(fa), g = lb(ga);
#pragma unroll
    for (int s = 0; s <= T[0]; ++s) {
      SBAR();
#pragma unroll
      for (int k = 0; k < 4; ++k)
        if (s >= D[k] && s - D[k] <= T[k]) acc[k] = mfma(G[T[k] - s + D[k]], F[s - D[k]], acc[k]);
      SBAR();
      G[T[0] - s] = *lp<u32x4>(g + 512 * (T[0] - s));
#pragma unroll
      for (int a = 0; a <= T[0]; ++a)
        if (a + (a <= T[2] ? 2 : a <= T[1] ? 1 : 0) == s) F[a] = trd2(f + 512 * a + 32, f + 512 * a);
    }
  }
}

#ifdef __HIP_DEVICE_COMPILE__
#define NOPK __attribute__((target("no-packed-fp32-ops")))
#else
#define NOPK
#endif
__global__ NOPK void __launch_bounds__(512, 2)
fused(const char* __restrict__ u, const u16* __restrict__ Are, const u16* __restrict__ Aim, const u16* __restrict__ Bre,
      const u16* __restrict__ Bim, const u16* __restrict__ Cre, const u16* __restrict__ Cim, const u16* __restrict__ ldt,
      const char* __restrict__ Dsk, const char* __restrict__ x, const char* __restrict__ wqkv, const char* __restrict__ wo,
      char* __restrict__ out, char* __restrict__ ws) {
  __shared__ __attribute__((aligned(16))) char lds_all[L_END];
  asm volatile("" ::"v"(lds_all), "s"(u), "s"(Are), "s"(Aim), "s"(Bre), "s"(Bim), "s"(Cre), "s"(Cim), "s"(ldt), "s"(Dsk), "s"(x), "s"(wqkv), "s"(wo), "s"(out), "s"(ws));
  const int b = blockIdx.x;
  const int hq = (b & 1) * 4 + ((b >> 3) & 3), qb = ((b >> 1) & 3) * 8 + (b >> 5);
  const int lane = threadIdx.x & 63, w = __builtin_amdgcn_readfirstlane(threadIdx.x >> 6);
  const int r = lane & 31, h = lane >> 5;
  const int q4 = (lane >> 2) & 3, pp = lane & 3, g1 = (lane >> 4) & 1;
  const int tb = qb >> 1, hp = 2 * hq + (qb & 1);
  ST(0);
  const lds_t wta = lb((8 * h + q4) * 64 + 32 * g1 + 8 * pp);
  const int cl = w >> 1, mh = w & 1, ch = hq * 64 + 2 * qb + cl;
  const unsigned pi = (ch * 128 + mh * 64 + lane) * 2;
  unsigned ev, l_dt, l_ds, l_ar, l_ai, l_br, l_bi, l_cr, l_ci;
  u32x4 ua, uc;
  if (w < 4) {
    ev = 0;
    if (lane < 2) {
      unsigned long long pc;
      asm volatile("s_getpc_b64 %0" : "=s"(pc));
      asm volatile("global_load_dword %0, %1, %2" : "+v"(ev) : "v"(min(lane * 128 + w * 32 + (b >> 3), CLN) * 128), "s"(pc) : "memory");
    }
    asm volatile("global_load_dword %0, %3, %4 sc1\n\tglobal_load_dwordx4 %1, %5, %6\n\tglobal_load_dwordx4 %2, %5, %7"
                 : "+v"(ev), "=&v"(ua), "=&v"(uc)
                 : "v"(0u), "s"(ws), "v"((lane >> 3) * 4096 + (lane & 7) * 16), "s"(u + (qb * 64 + w) * 1024 + hq * 128),
                   "s"(u + (qb * 64 + 32 + w) * 1024 + hq * 128) : "memory");
    asm volatile("global_load_ushort %0, %8, %9\n\tglobal_load_ushort %1, %8, %10\n\tglobal_load_ushort %2, %8, %11\n\t"
                 "global_load_ushort %3, %8, %12\n\tglobal_load_ushort %4, %8, %13\n\tglobal_load_ushort %5, %8, %14\n\t"
                 "global_load_ushort %6, %15, %16\n\tglobal_load_ushort %7, %15, %17"
                 : "=&v"(l_br), "=&v"(l_bi), "=&v"(l_cr), "=&v"(l_ci), "=&v"(l_ar), "=&v"(l_ai), "=&v"(l_dt), "=&v"(l_ds)
                 : "v"(pi), "s"(Bre), "s"(Bim), "s"(Cre), "s"(Cim), "s"(Are), "s"(Aim), "v"(ch * 2), "s"(ldt), "s"(Dsk) : "memory");
  }
  u64 tag;
  unsigned epoch;
  char* wa;
  __amdgpu_buffer_rsrc_t wsr;
  auto mktag = [&](unsigned e0) __attribute__((always_inline)) {
    epoch = __builtin_amdgcn_readfirstlane(e0) * 1664525u + 1013904223u;
    tag = ((u64)(epoch * 0x9E3779B1u + 0x7F4A7C15u) << 32) | epoch;
    wa = ws + WS_A0;
    wsr = __builtin_amdgcn_make_buffer_rsrc(wa, 0, WS_AREA, 0x00020000);
  };
  auto raise = [&](unsigned f, int i = 0) __attribute__((always_inline)) {
    if (lane == 0) __hip_atomic_store((AS1 u64*)(wa + f + (hq * 32 + qb) * 128 + i * 8), tag, __ATOMIC_RELAXED, __HIP_MEMORY_SCOPE_AGENT);
  };
  u32x4 q0, q1, q2, q3;
  const int head = 2 * hp + (w & 1), kq = (w >> 1) ^ KQX;
  const unsigned kvh = WS_KV + head * KV_HEAD;
  const unsigned koff = lane * 16;
  const int wl = w & 3;
  f32x16 pb0, pb1, pb2, o0, o1, o2, o3;
  const unsigned gfl = w < 4 ? FL_GV : FL_GK, gcf = w < 4 ? FL_C1 : FL_WF, gar = w < 4 ? L_GK : L_X;
  unsigned gc, f2o, pf, pg;
  u32x4 ad0, ad1, ad2;
  u64 f2v, tg;
  auto prep = [&]() __attribute__((always_inline)) {
    const unsigned woff = (lane >> 4) * 1024 + (((lane & 15) ^ ((lane >> 4) << 2)) << 4);
    ad0 = u32x4{koff, gar + wl * 5120 + koff, 0xDDDD, gfl + (wl ^ 2) * 4};
    ad1 = u32x4{woff, woff + 3072, woff + 6144, woff + 9216};
    ad2 = u32x4{gcf + pp * 4, gar + (wl ^ 2) * 5120 + koff, L_GK + (wl ^ 2) * 5120 + koff, gfl + wl * 4};
    f2v = f2o = WS_F2 + (h ? (2 * wl + (lane & 1)) * 32 + (qb ^ (pp >> 1)) : hq * 32 + 8 * wl + (lane & 7)) * 128 + (r >> 3) * 8;
    pf = WS_F2 + (hq * 32 + qb) * 128 + w * 8;
    tg = tag;
    asm volatile(ATT_INI_ASM : "={v[160:175]}"(pb0), "={v[176:191]}"(pb1), "={v[192:207]}"(pb2), "={v[64:79]}"(o0), "={v[80:95]}"(o1),
                 "={v[96:111]}"(o2), "={v[112:127]}"(o3), "+{v[240:243]}"(ad0), "+{v[244:247]}"(ad1), "+{v[248:251]}"(ad2), "+{v[210:211]}"(f2v), "+v"(f2o), "+{v208}"(pf), "+{v[212:213]}"(tg)
                 : "{s[42:43]}"(0x0000FFFF0000FFFFull), "{s[52:53]}"(0x3F803F803F803F80ull) : "scc");
  };
  auto first = [&]() __attribute__((always_inline)) {
    asm volatile(ATT_PRE_ASM : "+{v[160:175]}"(pb0), "+{v[176:191]}"(pb1), "+{v[192:207]}"(pb2)
                 : "{s[40:41]}"(wa + kvh + kq * 32768 + 4096), "{v[240:243]}"(ad0), "{s[42:43]}"(0x0000FFFF0000FFFFull) : "memory");
  };
  auto getq = [&]() __attribute__((always_inline)) {
    lwait(FL_Q + pp * 4);
    const lds_t a = lb(L_QF + (w & 1) * 4096 + (w & 2) * 1024 + koff), c = lb(L_QF + (w & 1) * 4096 + (~w & 2) * 1024 + koff);
    q0 = *lp<u32x4>(a); q1 = *lp<u32x4>(a + 1024); q2 = *lp<u32x4>(c); q3 = *lp<u32x4>(c + 1024);
  };
  const unsigned f1o = WS_F1 + (hq * 32 + (qb & 1) + 8 * kq + 2 * pp) * 128 + q4 * 8;

  if (__builtin_expect(w >= 4, KF)) {
    const int kw = w - 4, rot = ROT;
    unsigned e0;
    asm volatile("global_load_dword %0, %1, %2 sc1" : "=v"(e0) : "v"(0u), "s"(ws) : "memory");
    const unsigned row = 32 * kw + (lane >> 3);
    const unsigned xo0 = row * 1024 + (((lane & 7) ^ (lane >> 4)) << 4);
    const unsigned xo1 = (row + 8) * 1024 + (((lane & 7) ^ (lane >> 4) ^ 4) << 4);
    const unsigned wo0 = (16 * kw + (lane >> 2)) * 3072 + pp * 16;
    const char* xb = x + tb * 131072;
    const char* wb = wqkv + hp * 64;
    auto D0 = [&](int c, int i) __attribute__((always_inline)) {
      const unsigned d = (c & 3) * L_SLOT;
      if (i < 2) dma(wb + ((c + rot) & 7) * (64 * 3072) + (i + 1) * 1024, wo0, d + 16384 + i * 4096 + kw * 1024);
      else dma(xb + (((c + rot) & 7) << 7) + (i & 4) * 4096, i & 1 ? xo1 : xo0, d + kw * 4096 + (i - 2) * 1024);
    };
    auto D1 = [&](int c, int i) __attribute__((always_inline)) { D0(c, i); asm volatile("s_nop 1"); };
    auto DQ = [&](int i) __attribute__((always_inline)) {
      dma(wb + ((i + rot) & 7) * (64 * 3072), wo0, i * 4096 + kw * 1024);
      asm volatile("s_nop 1");
    };
    ST(14);
#pragma unroll
    for (int i = 0; i < CUM[0]; ++i) D0(i / 6, i % 6);
    ST(1);
    const int rs = (r & 19) | ((r & 4) << 1) | ((r & 8) >> 1);
    const unsigned xrow = 32 * kw + rs;
    const unsigned xa = xrow * 128 + ((h ^ ((xrow >> 1) & 7)) << 4);
    const lds_t xs[4] = {lb(xa), lb(xa ^ 32), lb(xa ^ 64), lb(xa ^ 96)};
    const lds_t xs2[4] = {lb(xa + 2 * L_SLOT), lb((xa ^ 32) + 2 * L_SLOT), lb((xa ^ 64) + 2 * L_SLOT), lb((xa ^ 96) + 2 * L_SLOT)};
    const lds_t wt2 = lb((unsigned)(size_t)wta + 2 * L_SLOT);
    u32x4 xr[32];
    f32x16 acc = {0}, acv = {0};
    __builtin_amdgcn_s_setprio(PRIO_Q);
#pragma unroll
    for (int c = 0; c < 8; ++c) {
      asm volatile("s_waitcnt vmcnt(%0) lgkmcnt(0)\n\ts_barrier" ::"n"(CUM[c] - 6 * c - 6) : "memory");
      if (c == 0) ST(18);
      if (c == SS) ST(15);
      if (c == 7) ST(19);
      u32x4 wk[4], wv[4];
      auto RD = [&](int s) __attribute__((always_inline)) {
        xr[4 * c + s] = *lp<u32x4>(((c & 2) ? xs2[s] : xs[s]) + (c & 1) * L_SLOT);
        lds_t a = ((c & 2) ? wt2 : wta) + (c & 1) * L_SLOT + 16384 + s * 1024;
        wk[s] = trd2(a, a + 256);
        SBAR();
        wv[s] = trd2(a + 4096, a + 4352);
        SBAR();
      };
      auto DX = [&](int i) __attribute__((always_inline)) {
        const int n = CUM[c] + i;
        if (n >= CUM[c + 1]) return;
        if (n < 48) D1(n / 6, n % 6); else DQ(n - 48);
      };
      auto GAP = [&](int i) __attribute__((always_inline)) {
        SBAR();
        if (i == 0) RD(2);
        else if (i == 1) RD(3);
        else DX(i - 2 + DE);
        SBAR();
      };
      RD(0); RD(1);
#pragma unroll
      for (int i = 0; i < DE; ++i) DX(i);
      SBAR();
#pragma unroll
      for (int s = 0; s < 4; ++s) {
        acc = mfma(wk[s], xr[4 * c + s], acc);
        GAP(2 * s);
        acv = mfma(xr[4 * c + s], wv[s], acv);
        GAP(2 * s + 1);
      }
      if (c == 5) { asm volatile("" : "+v"(e0)); mktag(e0); }
    }
    if (PV & 4) __builtin_amdgcn_s_setprio(0);
    {
      unsigned vo = WS_KV + 2 * hp * KV_HEAD + (4 * tb + kw) * 2048;
      unsigned vk = vo + (rs + 32 * h) * 16, vv = vo + g1 * KV_HEAD + 1024 + h * 512 + (lane & 15) * 16;
      __builtin_amdgcn_raw_buffer_store_b128(u32x4{pk(acc[0], acc[1]), pk(acc[2], acc[3]), pk(acc[4], acc[5]), pk(acc[6], acc[7])}, wsr, vk, 0, SC1);
      __builtin_amdgcn_raw_buffer_store_b128(u32x4{pk(acc[8], acc[9]), pk(acc[10], acc[11]), pk(acc[12], acc[13]), pk(acc[14], acc[15])}, wsr, vk + KV_HEAD, 0, SC1);
      __builtin_amdgcn_raw_buffer_store_b128(u32x4{pk(acv[0], acv[1]), pk(acv[2], acv[3]), pk(acv[4], acv[5]), pk(acv[6], acv[7])}, wsr, vv, 0, SC1);
      __builtin_amdgcn_raw_buffer_store_b128(u32x4{pk(acv[8], acv[9]), pk(acv[10], acv[11]), pk(acv[12], acv[13]), pk(acv[14], acv[15])}, wsr, vv + 256, 0, SC1);
    }
    ST(2);
    VMW(4);
    lset(FL_WQ + kw * 4);
    lwait(FL_WQ + pp * 4);
    ST(6);
    if (PV & 4) __builtin_amdgcn_s_setprio(PRIO_Q);
    acc = f32x16{0}; acv = f32x16{0};
    const char* f1p = wa + f1o;
    u64 f1v;
    {
      u32x4 wf[8];
      auto RQ = [&](int i) __attribute__((always_inline)) { lds_t a = wta + i * 1024; wf[i & 7] = trd2(a, a + 256); };
#pragma unroll
      for (int i = 0; i < QD; ++i) RQ(i);
#pragma unroll
      for (int i = 0; i < 32; ++i) {
        SBAR();
        if (i & 1) acv = mfma(wf[i & 7], xr[i], acv); else acc = mfma(wf[i & 7], xr[i], acc);
        SBAR();
        if (i == QF1) { VMW(0); raise(WS_F1, kw); ST(3); }
        if (i == QFL) f1v = __hip_atomic_load((const AS1 u64*)f1p, __ATOMIC_RELAXED, __HIP_MEMORY_SCOPE_AGENT);
        if (i + QD < 32) RQ(i + QD);
      }
    }
    if (PV & 1) __builtin_amdgcn_s_setprio(0);
    acc += acv;
    const float qs = 0.25f * 1.44269504088896341f;
    acc *= qs;
    lds_t qa = lb(L_QF + kw * 1024 + (rs + 32 * h) * 16);
    *lp<u32x4>(qa) = u32x4{pk(acc[0], acc[1]), pk(acc[2], acc[3]), pk(acc[4], acc[5]), pk(acc[6], acc[7])};
    *lp<u32x4>(qa + 4096) = u32x4{pk(acc[8], acc[9]), pk(acc[10], acc[11]), pk(acc[12], acc[13]), pk(acc[14], acc[15])};
    lset(FL_Q + kw * 4);
    ST(4);
    SBAR();
    if (QFL > 31) f1v = __hip_atomic_load((const AS1 u64*)f1p, __ATOMIC_RELAXED, __HIP_MEMORY_SCOPE_AGENT);
    SBAR();
    if (PV & 2) __builtin_amdgcn_s_setprio(0);
    prep();
    getq();
    lset(FL_C1 + kw * 4);
    ST(5);
    SBAR();
    if (!allv(f1v == tag)) poll(f1p, tag);
    CFENCE();
    first();
    __builtin_amdgcn_s_setprio(PRIO_KA);
  } else {
    const int ti = lane & 15, tq = lane >> 4;
    float aA = (float)(ti < 8 ? ti : ti < 11 ? 480 + 4 * ti : ti < 12 ? 8 : 1024 + (ti & 2) * 256 + (ti & 1) * 8), aB = (float)(8 * ti);
    const unsigned yb = L_VA + w * 8448, up = L_EX + cl * 4608, ul = yb + 4608;
    if (w == 0 && lane < 60) *lp<unsigned>(L_FL + lane * 4) = 0;
    if (lane < 4) { *lp<u32x4>(L_KF + cl * 4160 + lane * 16) = u32x4{0}; *lp<unsigned>(L_OV + cl * 144 + lane * 4) = 0; }
    if (lane < 32) *lp<u32x4>(up + lane * 16) = u32x4{0};
    const lds_t pr = lb(yb + tq * 64), pw = lb(yb + lane * 16);
    const lds_t uw0 = lb(ul + lane * 16), ur0 = lb(ul + q4 * 128 + (lane >> 4) * 32 + pp * 8);
    asm volatile("s_waitcnt vmcnt(8)" : "+v"(ev), "+v"(ua), "+v"(uc), "+v"(aA), "+v"(aB)::"memory");
    ST(14);
    mktag(ev);
    {
      *lp<u32x4>(uw0) = ua;
      *lp<u32x4>(uw0 + 1024) = uc;
      u32x4 t0 = trd2(ur0, ur0 + 512), t1 = trd2(ur0 + 1024, ur0 + 1536);
      unsigned vo = WS_UT + ((hq * 32 + qb) * 8 + 2 * w) * 1024 + lane * 16;
      __builtin_amdgcn_raw_buffer_store_b128(t0, wsr, vo, 0, SC1);
      __builtin_amdgcn_raw_buffer_store_b128(t1, wsr, vo + 1024, 0, SC1);
    }
    asm volatile("s_waitcnt vmcnt(2)" : "+v"(l_br), "+v"(l_bi), "+v"(l_cr), "+v"(l_ci), "+v"(l_ar), "+v"(l_ai), "+v"(l_dt), "+v"(l_ds)::"memory");
    {
      float dt = b2f(pk(__expf(b2f(l_dt)), 0.f));
      float br = b2f(l_br) * dt, bi = b2f(l_bi) * dt, cr = b2f(l_cr), ci = b2f(l_ci);
      float wr = cr * br - ci * bi, wi = cr * bi + ci * br;
      float mx = fmaxf(fabsf(wr), fabsf(wi)), mn = fminf(fabsf(wr), fabsf(wi));
      int ex = __builtin_amdgcn_frexp_expf(mx);
      float rt = __builtin_amdgcn_ldexpf(mn, -ex) * __builtin_amdgcn_rcpf(__builtin_amdgcn_frexp_mantf(mx));
      rt = mx > 0.f ? rt : 0.f;
      float r2 = rt * rt;
      float at = __builtin_fmaf(r2, 0x1.5a54b0p-9f, -0x1.f4b218p-7f);
      at = __builtin_fmaf(r2, at, 0x1.53f67ep-5f);
      at = __builtin_fmaf(r2, at, -0x1.2fa9aep-4f);
      at = __builtin_fmaf(r2, at, 0x1.b26364p-4f);
      at = __builtin_fmaf(r2, at, -0x1.22c1ccp-3f);
      at = __builtin_fmaf(r2, at, 0x1.99717ep-3f);
      at = __builtin_fmaf(r2, at, -0x1.5554c4p-2f);
      at = __builtin_fmaf(rt, r2 * at, rt) * 0.15915494309189535f;
      at = fabsf(wi) > fabsf(wr) ? 0.25f - at : at;
      at = wr < 0.f ? 0.5f - at : at;
      f32x4 pv;
      pv[0] = fmaxf(dt * b2f(l_ar) * 1.44269504088896341f, -1e30f);
      pv[1] = dt * b2f(l_ai) * 0.15915494309189535f;
      pv[1] -= __builtin_rintf(pv[1]);
      pv[2] = __log2f(mx) + 0.5f * __log2f(1.f + r2);
      pv[3] = __builtin_copysignf(at, wi);
      *lp<f32x4>(pw) = pv;
      f32x2 z = cexp(128.f * pv[0], 128.f * pv[1]);
      *lp<f32x4>(pw + 1024) = f32x4{z[0], -z[1], z[0] * z[0] - z[1] * z[1], 2.f * z[0] * z[1]};
    }
    f32x4 pm[16], pz[16];
    auto RP = [&](int e) __attribute__((always_inline)) {
      pm[e] = *lp<f32x4>(pr + (e >> 2) * 256 + (e & 3) * 16);
      pz[e] = *lp<f32x4>(pr + 1024 + (e >> 2) * 256 + (e & 3) * 16);
    };
    RP(0); RP(1);
    f32x4 kc[4] = {{0}, {0}, {0}, {0}};
    kc[0][0] = lane + mh == 0 ? b2f(l_ds) : 0.f;
    ST(1);
    const char* f0p = wa + WS_F0 + (hq * 32 + 8 * w + (lane & 7)) * 128 + (r >> 3) * 8;
    u64 f0v;
    u32x4 uv[2], fa[2], fb[2];
    bool early;
    const unsigned uo = WS_UT + (hq * 256 + 64 * w + (lane >> 1)) * 1024 + (2 * qb + (lane & 1)) * 16;
    auto expand = [&]() __attribute__((always_inline)) {
      lds_t pa = lb(L_VA + (w ^ 1) * 8448 + 4608 + lane * 16);
      f32x4 s0 = kc[0] + *lp<f32x4>(pa), s1 = kc[1] + *lp<f32x4>(pa + 1024);
      const lds_t kf = lb(L_KF + cl * 4160 + 64 + (ti + 16 * mh) * 16), kq = lb((unsigned)(size_t)kf + tq * 8);
      const unsigned p00 = pk(s0[0], s0[0]), p01 = pk(s0[0], s0[1]), p11 = pk(s0[1], s0[1]), p22 = pk(s0[2], s0[2]), p23 = pk(s0[2], s0[3]);
      const unsigned r00 = pk(s1[0], s1[0]), r01 = pk(s1[0], s1[1]), r11 = pk(s1[1], s1[1]), r22 = pk(s1[2], s1[2]), r33 = pk(s1[3], s1[3]);
      if (tq < 2) {
        *lp<u32x2>(kq) = u32x2{p01, p23};
        *lp<u32x2>(kq + 512) = u32x2{r00, r22};
      } else if (tq == 2) {
        *lp<u32x4>(kf + 1024) = u32x4{p00, p01, p11, p22};
        *lp<u32x4>(kf + 1536) = u32x4{r00, r01, r11, r22};
      } else {
        const bool odd = ti & 1;
        const unsigned p33 = pk(s0[3], s0[3]), r23 = pk(s1[2], s1[3]);
        *lp<u32x4>(kf + 2048) = u32x4{p00, p00, odd ? p11 : p01, p11};
        *lp<u32x4>(kf + 2560) = u32x4{r00, r00, odd ? r11 : r01, r11};
        *lp<u32x4>(kf + 3072) = u32x4{p22, p22, odd ? p33 : p23, p33};
        *lp<u32x4>(kf + 3584) = u32x4{r22, r22, odd ? r33 : r23, r33};
      }
      CFENCE();
      if (tq == 1 || tq == 2) *lp<u16>(kq + 510) = (u16)(tq == 1 ? r00 : r33);
    };
    f32x16 ya[4];
    auto zero = [&]() __attribute__((always_inline)) {
      asm volatile("s_nop 4\n\tv_mfma_f32_32x32x16_bf16 %0, %4, %4, 0\n\tv_mfma_f32_32x32x16_bf16 %1, %4, %4, 0\n\t"
                   "v_mfma_f32_32x32x16_bf16 %2, %4, %4, 0\n\tv_mfma_f32_32x32x16_bf16 %3, %4, %4, 0"
                   : "=&v"(ya[0]), "=&v"(ya[1]), "=&v"(ya[2]), "=&v"(ya[3]) : "v"(u32x4{0}));
    };
    auto putu = [&]() __attribute__((always_inline)) {
      lds_t uw = lb(L_EX + (lane & 1) * 4608 + 512 + (64 * w + (lane >> 1)) * 16);
      *lp<u32x4>(uw) = uv[0];
      *lp<u32x4>(uw + 512) = uv[1];
      lset(FL_TAB + w * 4);
    };
#pragma unroll
    for (int n = 0; n < 16; ++n) {
      if (n < 14) RP(n + 2);
      SBAR();
      {
        f32x4 p = pm[n], z = pz[n];
        f32x2 ea = cexp(__builtin_fmaf(p[0], aA, p[2]), __builtin_amdgcn_fractf(__builtin_fmaf(p[1], aA, p[3])));
        f32x2 eb = cexp(p[0] * aB, -(p[1] * aB));
        fa[0][n & 3] = pk(ea[0], ea[1]); fa[1][n & 3] = cmul(ea, z[2], z[3]);
        fb[0][n & 3] = pk(eb[0], eb[1]); fb[1][n & 3] = cmul(eb, z[0], z[1]);
      }
      if ((n & 3) == 3) {
#pragma unroll
        for (int i = 0; i < 4; ++i) kc[i] = mfma16(fa[i & 1], fb[i >> 1], kc[i]);
      }
      if (n == 15) {
        if (mh) { f32x4 t0 = kc[0], t1 = kc[1]; kc[0] = kc[2]; kc[1] = kc[3]; kc[2] = t0; kc[3] = t1; }
        *lp<f32x4>(uw0) = kc[2]; *lp<f32x4>(uw0 + 1024) = kc[3];
      }
      SBAR();
      if (n + 1 == -(F0_B)) { ST(15); VMW(0); raise(WS_F0, w); ST(16); }
#pragma unroll
      for (int c = 0; c < 8; ++c)
        if (KGU[c] == n + 1) {
          BAR();
          if (c == 7) ST(2);
          if (c == F0_B) { ST(15); VMW(0); raise(WS_F0, w); ST(16); }
          if (c == FV_B) f0v = __hip_atomic_load((const AS1 u64*)f0p, __ATOMIC_RELAXED, __HIP_MEMORY_SCOPE_AGENT);
          if (c == UC_B) {
            early = allv(f0v == tag);
            CFENCE();
            if (early) {
              uv[0] = __builtin_amdgcn_raw_buffer_load_b128(wsr, uo, 0, SC1);
              uv[1] = __builtin_amdgcn_raw_buffer_load_b128(wsr, uo, 32768, SC1);
            }
            ST(17);
          }
          if (n == 15 && c == EX_B) { __builtin_amdgcn_s_setprio(PRIO_X); expand(); }
          if (n == 15 && c == UW_B && early) putu();
          if (c == ZA_B) zero();
        }
    }
    if (!early) {
      poll(f0p, tag);
      uv[0] = __builtin_amdgcn_raw_buffer_load_b128(wsr, uo, 0, SC1);
      uv[1] = __builtin_amdgcn_raw_buffer_load_b128(wsr, uo, 32768, SC1);
      ST(3);
    }
    ST(4);
    if (EX_B > 7) { __builtin_amdgcn_s_setprio(PRIO_X); expand(); }
    if (UW_B > 7 || !early) putu();
    ST(5);
    if (ZA_B > 7) zero();
    lwait(FL_TAB + pp * 4);
    ST(6);
    __builtin_amdgcn_s_setprio(0);
    {
      const unsigned fa0 = L_KF + cl * 4160 + 128 + 32 * g1 + 8 * pp - 64 * h - 32 - 8 * q4;
      const int qq = (r >> 2) - 1 + h;
      const unsigned ga0 = up + 128 * (4 + (qq >> 1)) + 32 * (r & 3) + 16 * (qq & 1);
      if (mh) conv_wave<1>(fa0, ga0, ya); else conv_wave<0>(fa0, ga0, ya);
      ST(18);
      __builtin_amdgcn_s_setprio(PRIO_VR);
      u64 f1v = __hip_atomic_load((const AS1 u64*)(wa + f1o), __ATOMIC_RELAXED, __HIP_MEMORY_SCOPE_AGENT);
      const unsigned tiles = mh ? 0x01020506u : 0x00030407u;
      f32x4 y[4];
      lds_t yw = lb(yb + lane * 4), yr = lb(yb + h * 4160 + r * 32), yo = lb(yb + (lane >> 2) * 4160 + 1024 + pp * 4);
#pragma unroll
      for (int i = 0; i < 2; ++i) {
        CFENCE();
        if (lane < 16) *lp<u32x4>(yb + (lane >> 1) * 1040 + (lane & 1) * 1024) = u32x4{0};
        CFENCE();
#pragma unroll
        for (int k = 0; k < 2; ++k)
#pragma unroll
          for (int j = 0; j < 16; ++j) *lp<float>(yw + k * 4160 + (j & 3) * 1044 + (j >> 2) * 256) = ya[2 * i + k][j];
        CFENCE();
        y[2 * i] = (*lp<f32x4>(yr) + *lp<f32x4>(yr + 1040)) + (*lp<f32x4>(yr + 2080) + *lp<f32x4>(yr + 3120));
        y[2 * i + 1] = (*lp<f32x4>(yr + 16) + *lp<f32x4>(yr + 1056)) + (*lp<f32x4>(yr + 2096) + *lp<f32x4>(yr + 3136));
        if (lane < 8)
          *lp<float>(L_OV + cl * 144 + (((tiles >> (16 * i + 8 * (lane >> 2))) & 7) + 1) * 16 + pp * 4) =
              *lp<float>(yo + 1040) + *lp<float>(yo + 2080) + *lp<float>(yo + 3120);
        CFENCE();
      }
      getq();
      lset(FL_CONV + w * 4);
      prep();
      if (!allv(f1v == tag)) poll(wa + f1o, tag);
      CFENCE();
      first();
      {
        unsigned vo = WS_O + ((4 * tb + (w & 2)) * 32 + head) * 1024 + lane * 16;
        u32x4 ones = {~0u, ~0u, ~0u, ~0u};
        __builtin_amdgcn_raw_buffer_store_b128(ones, wsr, vo, 0, SC1);
        __builtin_amdgcn_raw_buffer_store_b128(ones, wsr, vo + 32768, 0, SC1);
      }
      lwait(FL_CONV + pp * 4);
#pragma unroll
      for (int i = 0; i < 2; ++i) {
        int tl = (tiles >> (16 * i + 8 * h)) & 7;
        f32x4 y0 = y[2 * i] + *lp<f32x4>(L_OV + cl * 144 + (r ? 0 : tl) * 16), y1 = y[2 * i + 1];
        __builtin_amdgcn_raw_buffer_store_b128(u32x4{pk(y0[0], y0[1]), pk(y0[2], y0[3]), pk(y1[0], y1[1]), pk(y1[2], y1[3])}, wsr,
                                               WS_YT + ch * 4096 + tl * 512 + r * 16, 0, SC1);
      }
    }
    ST(19);
    __builtin_amdgcn_s_setprio(PRIO_VA);
  }
  ST(7);

  {
    const char* kp = wa + kvh + kq * 32768 + 4096;
    const char* wp = wo + (hq & 3) * 256 + (w ^ 4) * 65536;
    asm volatile(ATT_LOOP_ASM
                 : "+{v[64:79]}"(o0), "+{v[80:95]}"(o1), "+{v[96:111]}"(o2), "+{v[112:127]}"(o3), "+{s[40:41]}"(kp), "+{s[46:47]}"(wp),
                   "+{v[160:175]}"(pb0), "+{v[176:191]}"(pb1), "+{v[192:207]}"(pb2), "+{v[248:251]}"(ad2), "+{v[210:211]}"(f2v), "+{v208}"(pf), "=&{v209}"(pg) ATT_STO
                 : "{v[48:51]}"(q0), "{v[52:55]}"(q1), "{v[56:59]}"(q2), "{v[60:63]}"(q3), "{v[240:243]}"(ad0), "{v[244:247]}"(ad1), "{v[212:213]}"(tg),
                   "{s[50:51]}"(wa), "{s[42:43]}"(0x0000FFFF0000FFFFull), "{s48}"((w ^ 4) * 16384)
                 : "scc", "vcc", "memory", "s44", CLOB ATT_STC);
  }
  gc = ad2[0];
  const lds_t gk = (lds_t)(size_t)ad2[1], mg = (lds_t)(size_t)ad2[2];
  struct Part { f32x4 a[4]; f32x2 l; };
  auto getp = [&](lds_t a) __attribute__((always_inline)) {
    return Part{{*lp<f32x4>(a + 1024), *lp<f32x4>(a + 2048), *lp<f32x4>(a + 3072), *lp<f32x4>(a + 4096)}, *lp<f32x2>(a)};
  };
  auto putp = [&](lds_t a, const f32x16& x0, const f32x16& x1) __attribute__((always_inline)) {
    *lp<f32x2>(a) = f32x2{x0[8], x1[8]};
    *lp<f32x4>(a + 1024) = f32x4{x0[0], x0[1], x0[2], x0[3]};
    *lp<f32x4>(a + 2048) = f32x4{x0[4], x0[5], x0[6], x0[7]};
    *lp<f32x4>(a + 3072) = f32x4{x1[0], x1[1], x1[2], x1[3]};
    *lp<f32x4>(a + 4096) = f32x4{x1[4], x1[5], x1[6], x1[7]};
  };
  auto addp = [&](const Part& z) __attribute__((always_inline)) {
#pragma unroll
    for (int i = 0; i < 8; ++i) { o0[i] += z.a[i >> 2][i & 3]; o1[i] += z.a[2 + (i >> 2)][i & 3]; }
    o0[8] += z.l[0]; o1[8] += z.l[1];
  };
  auto took = [&]() __attribute__((always_inline)) {
#pragma unroll
    for (int i = 0; i < 8; ++i) { o0[i] = pb1[4 + i]; o1[i] = i < 4 ? pb1[12 + i] : pb2[i - 4]; }
    o0[8] = pb1[2]; o1[8] = pb1[3];
  };
  ST(8);

  {
  int lane = threadIdx.x & 63;
  asm volatile("" : "+v"(lane));
  const int r = lane & 31, h = lane >> 5;
  const int q4 = (lane >> 2) & 3, pp = lane & 3, g1 = (lane >> 4) & 1;
  const int tt = 2 * qb + (hq >> 2);
  const lds_t fla = lb(L_FL + wl * 4), flp = lb(L_FL + pp * 4);
  auto lw = [&](lds_t p, int nap) __attribute__((always_inline)) {
#pragma unroll 1
    for (unsigned s = 0; s < SPIN_CAP; ++s) {
      if (allv(*(volatile AS3 unsigned*)p != 0)) break;
      if (nap == 1) __builtin_amdgcn_s_sleep(1); else __builtin_amdgcn_s_sleep(5);
    }
    CFENCE();
  };
  auto ls = [&](unsigned f) __attribute__((always_inline)) {
    asm volatile("s_waitcnt lgkmcnt(0)" ::: "memory");
    *(volatile AS3 unsigned*)(fla + (f - L_FL)) = 1;
  };
  u32x4 wf[16];
  auto RW = [&]() __attribute__((always_inline)) {
    const unsigned a = (w < 4) * 65536 + (4 * h + q4) * 256 + ((64 * wl + 32 * (g1 ^ (w >> 2)) + 8 * (((pp & 1) << 1) | (pp >> 1))) ^ (64 * q4));
#pragma unroll
    for (int g = 0; g < 4; ++g) {
      const lds_t c = lb(a + ((g + wl) & 3) * 16384);
#pragma unroll
      for (int i = 0; i < 4; ++i) wf[4 * g + i] = trd2(c + i * 4096, c + i * 4096 + 2048);
    }
  };
  auto late = [&]() __attribute__((always_inline)) {
    if (!allv(gc != 0)) {
      lw(lb(gcf + pp * 4), 1);
      putp((lds_t)(size_t)ad0[1], o2, o3);
      lset(gfl + (wl ^ 2) * 4);
    }
    lw(lb(gfl + wl * 4), 1);
    addp(getp(gk));
  };
  if (w >= 4) {
    if (__builtin_expect(!allv(pf != 0 && pg != 0 && gc != 0), 0)) {
      ST(17);
      if (!allv(gc != 0)) ST(16);
      late();
      lw(fla + (FL_MG - L_FL), 1);
      addp(getp(mg));
    } else {
      took();
      addp(Part{{pb0.lo.lo, pb0.lo.hi, pb0.hi.lo, pb0.hi.hi}, {pb1[0], pb1[1]}});
    }
    auto fin = [&](const f32x16& q, int i, bool any) __attribute__((always_inline)) {
      float f = __builtin_amdgcn_rcpf(q[8]);
      u32x4 o = {pk(f * q[0], f * q[1]), pk(f * q[2], f * q[3]), pk(f * q[4], f * q[5]), pk(f * q[6], f * q[7])};
#pragma unroll
      for (int k = 0; k < 4; ++k) if (any) o[k] = min(o[k], 0xFFFFFFFEu);
      __builtin_amdgcn_raw_buffer_store_b128(o, wsr, ad0[0] + WS_O + ((4 * tb + (w & 2)) * 32 + head) * 1024 + i * 32768, 0, SC1);
    };
    {
      float bg[4];
#pragma unroll
      for (int i = 0; i < 4; ++i) bg[i] = (fabsf(o0[i]) + fabsf(o0[i + 4])) + (fabsf(o1[i]) + fabsf(o1[i + 4]));
      float big = (bg[0] + bg[1]) + (bg[2] + bg[3]);
      big = __builtin_fmaf(o0[8] + o1[8], 1e7f, big);
      bool ok = big < 1e37f && fminf(o0[8], o1[8]) > 5e-20f;
      if (__builtin_expect(!allv(ok), 0)) {
        float m0 = -__builtin_inff(), m1 = m0;
        f32x16 n0 = {0}, n1 = {0};
#pragma unroll 1
        for (int j = 0; j < 64; ++j) {
          u32x4 kk = __builtin_amdgcn_raw_buffer_load_b128(wsr, koff, kvh + j * 2048, SC1);
          f32x16 a = mfma(kk, q0, n0), c = mfma(kk, q1, n0);
#pragma unroll
          for (int i = 0; i < 16; ++i) { m0 = fmaxf(m0, a[i]); m1 = fmaxf(m1, c[i]); }
        }
        m0 = maxh(m0);
        m1 = maxh(m1);
        o0 = o1 = n0;
        n0 -= m0; n1 -= m1;
#pragma unroll 1
        for (int j = 0; j < 64; ++j) {
          u32x4 kk = __builtin_amdgcn_raw_buffer_load_b128(wsr, koff, kvh + j * 2048, SC1);
          u32x4 v0 = __builtin_amdgcn_raw_buffer_load_b128(wsr, koff & ~256u, kvh + j * 2048 + 1024, SC1);
          u32x4 v1 = __builtin_amdgcn_raw_buffer_load_b128(wsr, koff & ~256u, kvh + j * 2048 + 1280, SC1);
          if (koff & 256) v0 = v1 = u32x4{0x3F803F80u, 0x3F803F80u, 0x3F803F80u, 0x3F803F80u};
          f32x16 a = mfma(kk, q0, n0), c = mfma(kk, q1, n1);
#pragma unroll
          for (int i = 0; i < 16; ++i) { a[i] = __builtin_amdgcn_exp2f(a[i]); c[i] = __builtin_amdgcn_exp2f(c[i]); }
          o0 = smf(v0, a.lo, o0); o0 = smf(v1, a.hi, o0);
          o1 = smf(v0, c.lo, o1); o1 = smf(v1, c.hi, o1);
        }
        fin(o0, 0, true);
        fin(o1, 1, true);
      } else {
        fin(o0, 0, false);
        SBAR();
        fin(o1, 1, false);
      }
    }
    ST(9);
    ls(FL_KD);
    lw(flp + (FL_KD - L_FL), 1);
    RW();
    ST(10);
    VMW(0);
    ls(FL_GO);
  } else {
    lw(flp + (FL_PW - L_FL), 1);
    ST(15);
    RW();
    SBAR();
    if (!allv(f2v == tag)) poll(wa + f2o, tag);
    CFENCE();
    u32x4 yv[2];
#pragma unroll
    for (int i = 0; i < 2; ++i)
      yv[i] = __builtin_amdgcn_raw_buffer_load_b128(wsr, WS_YT + (hq * 64 + 16 * w + (lane >> 3)) * 4096 + qb * 128 + (lane & 7) * 16, i * 32768, SC1);
    ls(FL_WF);
    ST(16);
    if (allv(pf != 0 && gc != 0)) took(); else late();
    putp(mg, o0, o1);
    ls(FL_MG);
    ST(3);
    lw(flp + (FL_WF - L_FL), 1);
    ST(17);
    __builtin_amdgcn_s_setprio(PRIO_VT);
    ST(9);
    const lds_t yw = lb(L_Y + w * 2048 + lane * 16), ya = lb(L_Y + w * 2048 + q4 * 128 + (lane >> 4) * 32 + pp * 8);
    *lp<u32x4>(yw) = yv[0];
    *lp<u32x4>(yw + 1024) = yv[1];
#pragma unroll
    for (int k = 0; k < 2; ++k) OST((qb * 64 + lane) * 2048 + hq * 128 + w * 32 + k * 16, trd2(ya + k * 1024, ya + k * 1024 + 512));
    ST(10);
    lw(fla + (FL_KD - L_FL), 5);
    lw(fla + (FL_GO - L_FL), 1);
  }
  ST(11);
  const unsigned sb = L_S + (w < 4) * 16384 + koff;
  const lds_t ol = lb(sb + wl * 4096), ob1 = lb(sb + ((wl + 1) & 3) * 4096), ob2 = lb(sb + ((wl + 2) & 3) * 4096), ob3 = lb(sb + ((wl + 3) & 3) * 4096);
  const lds_t xw = lb(L_X + w * 2048 + koff), xr = lb(L_X + (w ^ 4) * 2048 + koff);
  unsigned oo = (tt * 32 + r) * 2048 + 1024 + (hq & 3) * 256 + wl * 64 + (w >> 2) * 32 + h * 16;
  asm volatile("" : "+v"(oo));
  u32x4 v[16];
#pragma unroll 1
  for (unsigned s = 0; s < SPIN_CAP; ++s) {
    CFENCE();
    unsigned mx = 0;
#pragma unroll
    for (int i = 0; i < 4; ++i) v[i] = __builtin_amdgcn_raw_buffer_load_b128(wsr, koff + i * 1024, WS_O + (tt * 32 + 4 * (w ^ 4)) * 1024, SC1);
#pragma unroll
    for (int i = 0; i < 4; ++i) { *lp<u32x4>(ol + i * 1024) = v[i]; mx = max(max(mx, max(v[i][0], v[i][1])), max(v[i][2], v[i][3])); }
    if (allv(mx != 0xFFFFFFFFu)) break;
  }
  f32x16 acc = {0};
#pragma unroll
  for (int i = 0; i < 4; ++i) acc = mfma(wf[i], v[i], acc);
  BAR();
  ST(12);
#pragma unroll
  for (int i = 4; i < 16; ++i) v[i] = *lp<u32x4>((i < 8 ? ob1 : i < 12 ? ob2 : ob3) + (i & 3) * 1024);
  SBAR();
#pragma unroll
  for (int i = 4; i < 16; ++i) acc = mfma(wf[i], v[i], acc);
  *lp<f32x4>(xw) = f32x4{acc[8], acc[9], acc[10], acc[11]};
  *lp<f32x4>(xw + 1024) = f32x4{acc[12], acc[13], acc[14], acc[15]};
  BAR();
  f32x4 k0 = f32x4{acc[0], acc[1], acc[2], acc[3]} + *lp<f32x4>(xr), k1 = f32x4{acc[4], acc[5], acc[6], acc[7]} + *lp<f32x4>(xr + 1024);
  OST(oo, u32x4{pk(k0[0], k0[1]), pk(k0[2], k0[3]), pk(k1[0], k1[1]), pk(k1[2], k1[3])});
  if (b == 0 && threadIdx.x == 0) __hip_atomic_store((AS1 unsigned*)ws, epoch, __ATOMIC_RELAXED, __HIP_MEMORY_SCOPE_AGENT);
  ST(13);
  }
}

extern "C" void kernel_launch(void* const* d_in, const int* in_sizes, int n_in, void* d_out, int out_size, void* d_ws,
                              size_t ws_size, hipStream_t stream) {
  fused<<<256, 512, 0, stream>>>((const char*)d_in[0], (const u16*)d_in[1], (const u16*)d_in[2], (const u16*)d_in[3],
                                 (const u16*)d_in[4], (const u16*)d_in[5], (const u16*)d_in[6], (const u16*)d_in[7],
                                 (const char*)d_in[8], (const char*)d_in[9], (const char*)d_in[10], (const char*)d_in[11],
                                 (char*)d_out, (char*)d_ws);
}
```
